```python
import torch

def _softmax(x, axis=-1):
    return torch.softmax(x, dim=axis)
def _gelu(x):
    return torch.nn.functional.gelu(x, approximate="tanh")
def _silu(x):
    return torch.nn.functional.silu(x)
def _sigmoid(x):
    return torch.sigmoid(x)
def _layernorm(x, axis=-1, eps=1e-5):
    m = x.mean(dim=axis, keepdim=True)
    v = x.var(dim=axis, keepdim=True, unbiased=False)
    return (x - m) / torch.sqrt(v + eps)
def _scatter_add(tgt, idx, src, axis=0):
    return tgt.clone().index_add_(axis, idx, src)
def _segment_sum(src, seg, n):
    out = torch.zeros((n,) + src.shape[1:], dtype=src.dtype)
    return out.index_add_(0, seg, src)
def _pad(x, pw):
    flat = tuple(v for pair in reversed(pw) for v in pair)
    return torch.nn.functional.pad(x, flat)

kernel_name = 'parallel_sum__ssm_conv_h3__mixer_token_mlp__prompt'

def setup_inputs():

    _w = (lambda a: (lambda t: t.to(torch.bfloat16) if t.is_floating_point() else t)(torch.as_tensor(a)))
    def _A_setup():
        B, S, D = 1, 2048, 1024
        ks = 4
        return {
            "q": _w((lambda *s: torch.randn(*s, dtype=torch.bfloat16) * 0.05)(B, S, D)),
            "k": _w((lambda *s: torch.randn(*s, dtype=torch.bfloat16) * 0.2)(B, S, D)),
            "v": _w((lambda *s: torch.randn(*s, dtype=torch.bfloat16) * 0.2)(B, S, D)),
            "shift_k": _w((lambda *s: torch.randn(*s, dtype=torch.bfloat16) * 0.2)(D, ks) * 0.5),
            "ssm_k": _w((lambda *s: torch.randn(*s, dtype=torch.bfloat16) * 0.2)(D, S) * (1.0 / S ** 0.5)),
            "ssm_D": _w((lambda *s: torch.randn(*s, dtype=torch.bfloat16) * 0.2)(D)),
        }
    _A_in = _A_setup()

    def _B_setup():
        B, S, D, F = 1, 2048, 1024, 3584
        return {
            "x": _w((lambda *s: torch.randn(*s, dtype=torch.bfloat16) * 0.05)(B, S, D)),
            "w1": _w((lambda *s: torch.randn(*s, dtype=torch.bfloat16) * 0.4)(S, F) * (S ** -0.5)),
            "b1": _w((lambda *s: torch.randn(*s, dtype=torch.bfloat16) * 3.2)(F) * 0.02),
            "w2": _w((lambda *s: torch.randn(*s, dtype=torch.bfloat16) * 0.2)(F, S) * (F ** -0.5)),
            "b2": _w((lambda *s: torch.randn(*s, dtype=torch.bfloat16) * 0.4)(S) * 0.02),
        }
    _B_in = _B_setup()

    return {
        "A_q": _A_in["q"],
        "A_k": _A_in["k"],
        "A_v": _A_in["v"],
        "A_shift_k": _A_in["shift_k"],
        "A_ssm_k": _A_in["ssm_k"],
        "A_ssm_D": _A_in["ssm_D"],
        "B_x": _B_in["x"],
        "B_w1": _B_in["w1"],
        "B_b1": _B_in["b1"],
        "B_w2": _B_in["w2"],
        "B_b2": _B_in["b2"],
    }

def reference(A_q, A_k, A_v, A_shift_k, A_ssm_k, A_ssm_D, B_x, B_w1, B_b1, B_w2, B_b2):
    def _A_ref(q, k, v, shift_k, ssm_k, ssm_D):
        B, S, D = q.shape
        ks = shift_k.shape[-1]
        k_pad = _pad(k, ((0, 0), (ks - 1, 0), (0, 0)))
        k_shift = torch.zeros_like(k)
        for t in range(ks):
            k_shift = k_shift + k_pad[:, t:t + S, :] * shift_k[:, ks - 1 - t][None, None, :]
        kv = k_shift * v
        L = 2 * S
        kv_pad = _pad(kv, ((0, 0), (0, S), (0, 0)))
        kf_pad = _pad(ssm_k, ((0, 0), (0, S)))
        kv_f = (lambda x, axes=(-2,-1): torch.fft.rfft2(x.float(), dim=axes))(kv_pad[:, None, :, :], axes=(1, 2))
        kf_f = (lambda x, axes=(-2,-1): torch.fft.rfft2(x.float(), dim=axes))(kf_pad[:, None, :], axes=(1, 2))
        kf_f = (lambda x, *d: x.permute(*d))(kf_f, 1, 2, 0)
        y_f = kv_f * kf_f[None, :, :, :]
        y = (lambda x, s, axes=(-2,-1): torch.fft.irfft2(x, s=s, dim=axes).to(torch.bfloat16))(y_f, (1, L), axes=(1, 2))[:, 0, :S, :]
        y = y + kv * ssm_D[None, None, :]
        return q * y

    def _B_ref(x, w1, b1, w2, b2):
        B, S, D = x.shape
        h = _layernorm(x, axis=-1)
        u = torch.einsum("bsd,sf->bdf", h, w1) + b1
        u = _gelu(u)
        y = torch.einsum("bdf,fs->bds", u, w2) + b2
        return (lambda x, *d: x.permute(*d))(y, 0, 2, 1)

    x = A_q
    xn = _layernorm(x, axis=-1)
    return x + _A_ref(xn, A_k, A_v, A_shift_k, A_ssm_k, A_ssm_D) + _B_ref(xn, B_w1, B_b1, B_w2, B_b2)
```

```cpp
#include <hip/hip_runtime.h>
#include <hip/hip_bf16.h>
#include <utility>
#define ABL 0

typedef short    s16x8 __attribute__((ext_vector_type(8)));
typedef short    s16x4 __attribute__((ext_vector_type(4)));
typedef float    f32x4 __attribute__((ext_vector_type(4)));
typedef float    f32x2 __attribute__((ext_vector_type(2)));
typedef unsigned u32x4 __attribute__((ext_vector_type(4)));
typedef unsigned u32x2 __attribute__((ext_vector_type(2)));
typedef unsigned short u16;
#define LDS_AS __attribute__((address_space(3)))
#define DEVINL __device__ __forceinline__

constexpr int S_ = 2048, D_ = 1024, F_ = 3584;

constexpr size_t WS_HA   = 0;
constexpr size_t WS_KVT  = WS_HA  + (size_t)S_ * D_ * 2;
constexpr size_t WS_YA   = WS_KVT + (size_t)S_ * D_ * 2;
constexpr size_t WS_UA   = WS_YA  + (size_t)S_ * D_ * 2;
constexpr size_t WS_STAT = WS_UA  + (size_t)D_ * F_ * 2;

DEVINL float bf2f(unsigned v16) { return __uint_as_float(v16 << 16); }
DEVINL float bflo(unsigned v) { return __uint_as_float(v << 16); }
DEVINL float bfhi(unsigned v) { return __uint_as_float(v & 0xffff0000u); }
DEVINL unsigned pack_bf16(float lo, float hi) {
  typedef __bf16 bf2 __attribute__((ext_vector_type(2)));
  f32x2 v = {lo, hi};
  bf2 r = __builtin_convertvector(v, bf2);
  return __builtin_bit_cast(unsigned, r);
}
DEVINL float rbf(float x) { return bflo(pack_bf16(x, x)); }

template <int N> DEVINL void wait_vm() { asm volatile("s_waitcnt vmcnt(%0)" ::"n"(N) : "memory"); }
template <int I> struct IC { static constexpr int v = I; };
template <int B, int E, typename Fn> DEVINL void static_for(Fn&& f) {
  if constexpr (B < E) { f(IC<B>{}); static_for<B + 1, E>(f); }
}
DEVINL float wave_sum(float v) {
#pragma unroll
  for (int o = 32; o > 0; o >>= 1) v += __shfl_xor(v, o, 64);
  return v;
}
DEVINL s16x4 tr_read(const LDS_AS char* p) {
  return __builtin_amdgcn_ds_read_tr16_b64_v4i16((LDS_AS s16x4*)p);
}
template <int OFF> DEVINL void gload16(u32x4& dst, unsigned voff, const char* sbase) {
  asm volatile("global_load_dwordx4 %0, %1, %2 offset:%3" : "=v"(dst) : "v"(voff), "s"(sbase), "n"(OFF));
}
template <int OFF> DEVINL void glds16(unsigned voff, const char* sbase, unsigned ldsaddr) {
  asm volatile("s_mov_b32 m0, %2\n\ts_nop 0\n\tglobal_load_lds_dwordx4 %0, %1 offset:%3" ::"v"(voff), "s"(sbase), "s"(ldsaddr), "n"(OFF) : "memory");
}
DEVINL void pin4(u32x4& a, u32x4& b, u32x4& c, u32x4& d) { asm volatile("" : "+v"(a), "+v"(b), "+v"(c), "+v"(d)); }
DEVINL s16x8 cat8(s16x4 a, s16x4 b) { return __builtin_shufflevector(a, b, 0, 1, 2, 3, 4, 5, 6, 7); }

#define STAMPIT(ws, k, i)
__global__ __launch_bounds__(512) void k_prologue(const u16* __restrict__ x, const u16* __restrict__ kin,
                                                  const u16* __restrict__ vin, const u16* __restrict__ shk,
                                                  char* __restrict__ ws) {
  __shared__ __attribute__((aligned(16))) u16 sh[8 * 1024];
  const int b = blockIdx.x, tid = threadIdx.x, w = tid >> 6, lane = tid & 63;
  const int s0 = b * 8;
  STAMPIT(ws, 0, 0);
  const u16* xr = x + (size_t)(s0 + w) * D_;
  u32x4 xa = *(const u32x4*)(xr + lane * 8);
  u32x4 xb = *(const u32x4*)(xr + 512 + lane * 8);
  unsigned kk[11], vv[8];
#pragma unroll
  for (int i = 0; i < 11; ++i) {
    int s = s0 - 3 + i;
    kk[i] = (s >= 0) ? *(const unsigned*)(kin + (size_t)s * D_ + 2 * tid) : 0u;
  }
#pragma unroll
  for (int i = 0; i < 8; ++i) vv[i] = *(const unsigned*)(vin + (size_t)(s0 + i) * D_ + 2 * tid);
  u32x4 sk = *(const u32x4*)(shk + (size_t)tid * 8);

  float f[16];
#pragma unroll
  for (int i = 0; i < 4; ++i) { f[2 * i] = bflo(xa[i]); f[2 * i + 1] = bfhi(xa[i]); f[8 + 2 * i] = bflo(xb[i]); f[9 + 2 * i] = bfhi(xb[i]); }
  float m1b, sd1b;
#pragma unroll
  for (int pass = 0; pass < 2; ++pass) {
    float s = 0.f;
#pragma unroll
    for (int i = 0; i < 16; ++i) s += f[i];
    float mean = wave_sum(s) * (1.0f / D_);
    float q = 0.f;
#pragma unroll
    for (int i = 0; i < 16; ++i) { float dlt = f[i] - mean; q += dlt * dlt; }
    float var = wave_sum(q) * (1.0f / D_);
    float mb = rbf(mean), vb = rbf(var);
    float sd = rbf(sqrtf(rbf(vb + 1e-5f)));
#pragma unroll
    for (int i = 0; i < 16; ++i) f[i] = rbf(rbf(f[i] - mb) / sd);
    if (pass == 0) { m1b = mb; sd1b = sd; }
  }
  {
    u32x4 ha, hb;
#pragma unroll
    for (int i = 0; i < 4; ++i) { ha[i] = pack_bf16(f[2 * i], f[2 * i + 1]); hb[i] = pack_bf16(f[8 + 2 * i], f[9 + 2 * i]); }
    *(u32x4*)(sh + w * 1024 + lane * 8) = ha;
    *(u32x4*)(sh + w * 1024 + 512 + lane * 8) = hb;
    if (lane == 0) { f32x2 st = {m1b, 1.0f / sd1b}; *(f32x2*)(ws + WS_STAT + (size_t)(s0 + w) * 8) = st; }
  }
  {
    float w0[4] = {bflo(sk[0]), bfhi(sk[0]), bflo(sk[1]), bfhi(sk[1])};
    float w1[4] = {bflo(sk[2]), bfhi(sk[2]), bflo(sk[3]), bfhi(sk[3])};
    float r0[8], r1[8];
#pragma unroll
    for (int i = 0; i < 8; ++i) {
      float a0 = 0.f, a1 = 0.f;
#pragma unroll
      for (int t = 0; t < 4; ++t) {
        a0 = rbf(a0 + rbf(bflo(kk[i + t]) * w0[3 - t]));
        a1 = rbf(a1 + rbf(bfhi(kk[i + t]) * w1[3 - t]));
      }
      r0[i] = a0 * bflo(vv[i]);
      r1[i] = a1 * bfhi(vv[i]);
    }
    u32x4 o0, o1;
#pragma unroll
    for (int i = 0; i < 4; ++i) { o0[i] = pack_bf16(r0[2 * i], r0[2 * i + 1]); o1[i] = pack_bf16(r1[2 * i], r1[2 * i + 1]); }
    u32x4* dst = (u32x4*)(ws + WS_KVT) + (size_t)b * 1024 + 2 * tid;
    dst[0] = o0; dst[1] = o1;
  }
  STAMPIT(ws, 0, 1);
  __syncthreads();
  {
    unsigned c[8];
#pragma unroll
    for (int i = 0; i < 8; ++i) c[i] = *(const unsigned*)(sh + i * 1024 + 2 * tid);
    u32x4 o0, o1;
#pragma unroll
    for (int i = 0; i < 4; ++i) {
      o0[i] = __builtin_amdgcn_perm(c[2 * i + 1], c[2 * i], 0x05040100u);
      o1[i] = __builtin_amdgcn_perm(c[2 * i + 1], c[2 * i], 0x07060302u);
    }
    const int d = 2 * tid, dblk = d >> 5, n = (d >> 2) & 1, cc = 4 * ((d >> 3) & 3) + (d & 3);
    const int kstep = b >> 2, g = b & 3;
    u32x4* dst = (u32x4*)(ws + WS_HA) + ((size_t)(dblk * 64 + kstep) * 2 + n) * 64 + cc + 16 * g;
    dst[0] = o0; dst[1] = o1;
  }
  STAMPIT(ws, 0, 2);
}

constexpr int CV_KF = 4160;
constexpr int CV_XP = 1152;
constexpr int CV_WAVE = CV_KF + 4 * CV_XP;
constexpr int CV_LDS = 4 * CV_WAVE;

DEVINL void conv_block(LDS_AS char* smem, int blk, int tid, const u16* __restrict__ ssmk, const u16* __restrict__ ssmD,
                       char* __restrict__ ws) {
  const int w = tid >> 6, lane = tid & 63;
  const int d = blk * 4 + w;
  LDS_AS char* kf = smem + w * CV_WAVE;
  LDS_AS char* xp = kf + CV_KF;
  STAMPIT(ws, 1, 0);
  u32x4 kfv[4], kvv[4];
#pragma unroll
  for (int i = 0; i < 4; ++i) kfv[i] = *(const u32x4*)(ssmk + (size_t)d * S_ + (i * 64 + lane) * 8);
#pragma unroll
  for (int i = 0; i < 4; ++i) kvv[i] = *((const u32x4*)(ws + WS_KVT) + (size_t)(i * 64 + lane) * 1024 + d);
  const float Dd = bf2f(ssmD[d]);
  if (lane < 8) *(LDS_AS unsigned*)(kf + lane * 4) = 0u;
  {
    u32x2 z = {0u, 0u};
    *(LDS_AS u32x2*)(xp + (lane >> 4) * CV_XP + (lane & 15) * 8) = z;
  }
#pragma unroll
  for (int i = 0; i < 4; ++i) *(LDS_AS u32x4*)(kf + 32 + (i * 64 + lane) * 16) = kfv[i];
#pragma unroll
  for (int i = 0; i < 4; ++i) {
    const int sb = i * 64 + lane;
    unsigned p0 = __builtin_amdgcn_perm(kvv[i][2], kvv[i][0], 0x05040100u);
    unsigned p1 = __builtin_amdgcn_perm(kvv[i][2], kvv[i][0], 0x07060302u);
    unsigned p2 = __builtin_amdgcn_perm(kvv[i][3], kvv[i][1], 0x05040100u);
    unsigned p3 = __builtin_amdgcn_perm(kvv[i][3], kvv[i][1], 0x07060302u);
    *(LDS_AS unsigned*)(xp + 0 * CV_XP + 128 + sb * 4) = p0;
    *(LDS_AS unsigned*)(xp + 1 * CV_XP + 128 + sb * 4) = p1;
    *(LDS_AS unsigned*)(xp + 2 * CV_XP + 128 + sb * 4) = p2;
    *(LDS_AS unsigned*)(xp + 3 * CV_XP + 128 + sb * 4) = p3;
  }
  __builtin_amdgcn_s_waitcnt(0xC07F);
  __builtin_amdgcn_wave_barrier();
  STAMPIT(ws, 1, 1);
  const int g = lane >> 4, q = (lane >> 2) & 3, p = lane & 3, n = lane & 15;
  s16x8 af[16];
  {
    const LDS_AS char* a0 = kf + 2 * (4 * (8 * g + 7 - q) + 4 + 4 * p);
#pragma unroll
    for (int st = 0; st < 16; ++st) {
      s16x4 lo = tr_read(a0 + st * 256);
      s16x4 hi = tr_read(a0 + st * 256 - 32);
      af[st] = cat8(lo, hi);
    }
  }
  f32x4 acc[4][8];
#pragma unroll
  for (int ph = 0; ph < 4; ++ph)
#pragma unroll
    for (int nu = 0; nu < 8; ++nu) acc[ph][nu] = (f32x4){0.f, 0.f, 0.f, 0.f};
  const LDS_AS char* b0 = xp + 2 * (64 + 4 * n - 8 * g - 4);
#pragma unroll
  for (int beta = -1; beta <= 14; ++beta) {
#pragma unroll
    for (int ph = 0; ph < 4; ++ph) {
      const LDS_AS char* bp = b0 + ph * CV_XP + beta * 64;
      s16x4 lo = *(const LDS_AS s16x4*)(bp);
      s16x4 hi = *(const LDS_AS s16x4*)(bp + 8);
      s16x8 bf = cat8(lo, hi);
#pragma unroll
      for (int nu = 0; nu < 8; ++nu) {
        const int step = 2 * nu - beta;
        if (step >= 0 && step <= 15)
          acc[ph][nu] = __builtin_amdgcn_mfma_f32_16x16x32_bf16(af[step], bf, acc[ph][nu], 0, 0, 0);
      }
    }
  }
  STAMPIT(ws, 1, 2);
  const int src = ((lane >= 16) ? (lane - 16) : (lane == 0 ? 63 : lane + 47)) * 4;
  float prev0 = 0.f, prev1 = 0.f, prev2 = 0.f;
  LDS_AS char* ob = kf;
#pragma unroll
  for (int nu = 0; nu < 8; ++nu) {
    float c0 = acc[1][nu][3] + acc[2][nu][2] + acc[3][nu][1];
    float c1 = acc[2][nu][3] + acc[3][nu][2];
    float c2 = acc[3][nu][3];
    float p0 = __int_as_float(__builtin_amdgcn_ds_bpermute(src, __float_as_int(c0)));
    float p1 = __int_as_float(__builtin_amdgcn_ds_bpermute(src, __float_as_int(c1)));
    float p2 = __int_as_float(__builtin_amdgcn_ds_bpermute(src, __float_as_int(c2)));
    float a0 = (lane == 0) ? prev0 : p0, a1 = (lane == 0) ? prev1 : p1, a2 = (lane == 0) ? prev2 : p2;
    prev0 = p0; prev1 = p1; prev2 = p2;
    float y[4];
    y[0] = acc[0][nu][0] + a0;
    y[1] = acc[0][nu][1] + acc[1][nu][0] + a1;
    y[2] = acc[0][nu][2] + acc[1][nu][1] + acc[2][nu][0] + a2;
    y[3] = acc[0][nu][3] + acc[1][nu][2] + acc[2][nu][1] + acc[3][nu][0];
#pragma unroll
    for (int r = 0; r < 4; ++r) {
      float kvv_ = bf2f(*(const LDS_AS u16*)(xp + r * CV_XP + 128 + 2 * (64 * nu + 4 * n + g)));
      y[r] = rbf(y[r]) + rbf(kvv_ * Dd);
    }
    u32x2 o = {pack_bf16(y[0], y[1]), pack_bf16(y[2], y[3])};
    *(LDS_AS u32x2*)(ob + 2 * (256 * nu + 16 * n + 4 * g)) = o;
  }
  __syncthreads();
  STAMPIT(ws, 1, 3);
  {
    u32x4 c[4];
#pragma unroll
    for (int i = 0; i < 4; ++i) c[i] = *(const LDS_AS u32x4*)(smem + i * CV_WAVE + tid * 16);
    u32x4* dst = (u32x4*)(ws + WS_YA) + (size_t)blk * 1024 + tid * 4;
#pragma unroll
    for (int j = 0; j < 4; ++j) {
      u32x4 o;
      o[0] = __builtin_amdgcn_perm(c[1][j], c[0][j], 0x05040100u);
      o[1] = __builtin_amdgcn_perm(c[3][j], c[2][j], 0x05040100u);
      o[2] = __builtin_amdgcn_perm(c[1][j], c[0][j], 0x07060302u);
      o[3] = __builtin_amdgcn_perm(c[3][j], c[2][j], 0x07060302u);
      dst[j] = o;
    }
  }
  STAMPIT(ws, 1, 4);
}

__global__ __launch_bounds__(256) void k_conv(const u16* __restrict__ ssmk, const u16* __restrict__ ssmD, char* __restrict__ ws) {
  extern __shared__ __attribute__((aligned(16))) char dsm[];
  const int b = blockIdx.x;
  const int blk = (b & 7) * 32 + (b >> 3);
  conv_block((LDS_AS char*)dsm, blk, threadIdx.x, ssmk, ssmD, ws);
}

template <int NWT, bool G1, int NS, int NST>
DEVINL void gemm_main(LDS_AS char* smem, const char* __restrict__ Wg  , const size_t wrow  ,
                      const char* __restrict__ Xg  , unsigned xoff  ,
                      f32x4 (&acc)[NWT][2], int w, int lane) {
  constexpr int ROWB = NWT * 32;
  constexpr int STB = 64 * ROWB;
  constexpr int NG = STB / 1024 / 4;
  constexpr int OPS = NG + 4;
  constexpr int NF = 2 * NWT;
  constexpr int LA = 6;
  static_assert(NST % NS == 0, "");
  const int g = lane >> 4, q = (lane >> 2) & 3, p = lane & 3;
  unsigned woff[NG];
#pragma unroll
  for (int i = 0; i < NG; ++i) {
    int row, cp;
    if (G1) { row = 16 * w + 4 * i + (lane >> 4); cp = lane & 15; }
    else    { row = 16 * w + 8 * i + (lane >> 3); cp = lane & 7; }
    int key = G1 ? ((row & 3) | (((row >> 3) & 1) << 2)) : ((row >> 1) & 3);
    int sig = (cp >> 1) ^ key;
    woff[i] = (unsigned)(row * wrow) + 32 * sig + 16 * (cp & 1) - i * 1024;
  }
  const unsigned ldsw = (unsigned)(size_t)smem + w * NG * 1024;
  unsigned roff[NWT];
#pragma unroll
  for (int m = 0; m < NWT; ++m) {
    int row = G1 ? (8 * g + q) : (4 * g + q);
    int key = G1 ? (q | ((g & 1) << 2)) : ((row >> 1) & 3);
    roff[m] = row * ROWB + 32 * (m ^ key) + 8 * p;
  }
  constexpr int RSTEP = G1 ? 4 * ROWB : 16 * ROWB;
  u32x4 xf[NS][4];
  s16x8 wf[8];

  auto issue1 = [&](int st, auto SL, auto OP) {
    constexpr int sl = decltype(SL)::v, op = decltype(OP)::v;
    if constexpr (op < NG) {
      glds16<op * 1024>(woff[op], Wg + (size_t)st * 64 * wrow, ldsw + sl * STB);
    } else {
      gload16<(op - NG) * 1024>(xf[sl][op - NG], xoff, Xg + (size_t)st * 4096);
    }
  };
  auto readf = [&](auto SL, auto J, auto R) {
    constexpr int sl = decltype(SL)::v, j = decltype(J)::v, r = decltype(R)::v;
    constexpr int ks = j / NWT, m = j % NWT;
    const LDS_AS char* a = smem + sl * STB + ks * 32 * ROWB + roff[m];
    wf[r] = cat8(tr_read(a), tr_read(a + RSTEP));
  };
  auto mma = [&](auto SL, auto J, auto R) {
    constexpr int sl = decltype(SL)::v, j = decltype(J)::v, r = decltype(R)::v;
    constexpr int ks = j / NWT, m = j % NWT;
#pragma unroll
    for (int n = 0; n < 2; ++n) {
      s16x8 xv = __builtin_bit_cast(s16x8, xf[sl][ks * 2 + n]);
      if (G1) acc[m][n] = __builtin_amdgcn_mfma_f32_16x16x32_bf16(wf[r], xv, acc[m][n], 0, 0, 0);
      else    acc[m][n] = __builtin_amdgcn_mfma_f32_16x16x32_bf16(xv, wf[r], acc[m][n], 0, 0, 0);
    }
  };
  auto pin = [&](auto SL) {
    constexpr int sl = decltype(SL)::v;
    pin4(xf[sl][0], xf[sl][1], xf[sl][2], xf[sl][3]);
  };
  auto stage = [&](auto I, auto ISSUE, auto WAITN, int st_issue) {
    constexpr int i = decltype(I)::v, nx = (i + 1) % NS, pv = (i + NS - 1) % NS;
    constexpr int waitn = decltype(WAITN)::v;
    constexpr bool do_issue = decltype(ISSUE)::v;
    if constexpr (waitn >= 0) {
      wait_vm<waitn * OPS>();
      pin(IC<nx>{});
      __builtin_amdgcn_s_barrier();
      __builtin_amdgcn_sched_barrier(0);
    }
    static_for<0, NF>([&](auto J) {
      constexpr int j = decltype(J)::v, ja = j + LA;
      if constexpr (ja < NF) readf(IC<i>{}, IC<ja>{}, IC<(ja) % 8>{});
      else if constexpr (waitn >= 0) readf(IC<nx>{}, IC<ja - NF>{}, IC<(ja) % 8>{});
      if constexpr (do_issue && j < OPS) issue1(st_issue, IC<pv>{}, J);
      mma(IC<i>{}, J, IC<j % 8>{});
      __builtin_amdgcn_sched_barrier(0);
    });
  };

  static_for<0, NS - 1>([&](auto I) { static_for<0, OPS>([&](auto OP) { issue1(decltype(I)::v, I, OP); }); });
  wait_vm<(NS - 2) * OPS>();
  pin(IC<0>{});
  __builtin_amdgcn_s_barrier();
  __builtin_amdgcn_sched_barrier(0);
  static_for<0, LA>([&](auto J) { readf(IC<0>{}, J, J); });
  __builtin_amdgcn_sched_barrier(0);

  for (int tb = 0; tb < NST - NS; tb += NS) {
    static_for<0, NS>([&](auto I) { stage(I, IC<1>{}, IC<NS - 3>{}, tb + decltype(I)::v + NS - 1); });
  }
  static_for<0, NS>([&](auto I) {
    constexpr int i = decltype(I)::v;
    constexpr int outst = (i == NS - 1) ? -1 : ((i == 0) ? (NS - 3) : (NS - 2 - i));
    stage(I, IC<(i == 0)>{}, IC<outst>{}, NST - 1);
  });
}

constexpr int G1_NS = 8, G2_NS = 8;
constexpr int G1_LDS = G1_NS * 64 * 256, G2_LDS = G2_NS * 64 * 128;

__global__ __launch_bounds__(256, 1) void k_gemm1(const u16* __restrict__ w1, const u16* __restrict__ b1, char* __restrict__ ws) {
  extern __shared__ __attribute__((aligned(16))) char dsm[];
  const int b = blockIdx.x, xcd = b & 7, j = b >> 3;
  const int dt = (xcd & 1) * 4 + (j & 3), ft = (xcd >> 1) * 7 + (j >> 2);
  const int tid = threadIdx.x, lane = tid & 63;
  const int w = __builtin_amdgcn_readfirstlane(tid >> 6);
  f32x4 acc[8][2];
#pragma unroll
  for (int m = 0; m < 8; ++m) { acc[m][0] = (f32x4){0, 0, 0, 0}; acc[m][1] = (f32x4){0, 0, 0, 0}; }
  const char* Xg = ws + WS_HA + (size_t)dt * 4 * 64 * 2048;
  const unsigned xoff = (unsigned)(tid >> 6) * (64 * 2048) + lane * 16;
  STAMPIT(ws, 2, 0);
  gemm_main<8, true, G1_NS, 32>((LDS_AS char*)dsm, (const char*)w1 + ft * 256, (size_t)F_ * 2, Xg, xoff, acc, w, lane);
  STAMPIT(ws, 2, 1);
  const int g = lane >> 4;
  const int dblk = dt * 4 + (tid >> 6);
  u32x4* dst = (u32x4*)(ws + WS_UA) + ((size_t)(dblk * 112 + ft * 4) * 2) * 64 + lane;
#pragma unroll
  for (int i = 0; i < 4; ++i) {
    u32x2 bb0 = *(const u32x2*)(b1 + ft * 128 + 32 * i + 4 * g);
    u32x2 bb1 = *(const u32x2*)(b1 + ft * 128 + 32 * i + 16 + 4 * g);
    float bias[8] = {bflo(bb0[0]), bfhi(bb0[0]), bflo(bb0[1]), bfhi(bb0[1]), bflo(bb1[0]), bfhi(bb1[0]), bflo(bb1[1]), bfhi(bb1[1])};
#pragma unroll
    for (int n = 0; n < 2; ++n) {
      float r[8];
#pragma unroll
      for (int e = 0; e < 8; ++e) {
        float t = acc[2 * i + (e >> 2)][n][e & 3] + bias[e];
        float z = t * (-2.3022081985378543f + -0.10294324068f * t * t);
        float ex = __builtin_amdgcn_exp2f(z);
        r[e] = t * __builtin_amdgcn_rcpf(1.0f + ex);
      }
      u32x4 o = {pack_bf16(r[0], r[1]), pack_bf16(r[2], r[3]), pack_bf16(r[4], r[5]), pack_bf16(r[6], r[7])};
      dst[(i * 2 + n) * 64] = o;
    }
  }
  STAMPIT(ws, 2, 2);
}

__global__ __launch_bounds__(256, 1) void k_gemm2(const u16* __restrict__ w2, const u16* __restrict__ b2, const u16* __restrict__ x,
                                                  const char* __restrict__ ws, u16* __restrict__ out) {
  extern __shared__ __attribute__((aligned(16))) char dsm[];
  const int b = blockIdx.x, xcd = b & 7, j = b >> 3;
  const int dt = (xcd & 1) * 4 + (j & 3), stl = (xcd >> 1) * 8 + (j >> 2);
  const int tid = threadIdx.x, lane = tid & 63;
  const int w = __builtin_amdgcn_readfirstlane(tid >> 6);
  f32x4 acc[4][2];
#pragma unroll
  for (int m = 0; m < 4; ++m) { acc[m][0] = (f32x4){0, 0, 0, 0}; acc[m][1] = (f32x4){0, 0, 0, 0}; }
  const char* Xg = ws + WS_UA + (size_t)dt * 4 * 112 * 2048;
  const unsigned xoff = (unsigned)(tid >> 6) * (112 * 2048) + lane * 16;
  STAMPIT(ws, 3, 0);
  gemm_main<4, false, G2_NS, 56>((LDS_AS char*)dsm, (const char*)w2 + stl * 128, (size_t)S_ * 2, Xg, xoff, acc, w, lane);
  STAMPIT(ws, 3, 1);
  const int g = lane >> 4, n = lane & 15;
  const int d0 = dt * 128 + (tid >> 6) * 32 + 8 * g;
#pragma unroll
  for (int nt = 0; nt < 4; ++nt) {
    const int s = stl * 64 + 16 * nt + n;
    u32x4 xv = *(const u32x4*)(x + (size_t)s * D_ + d0);
    u32x2 ya0 = *(const u32x2*)(ws + WS_YA + ((size_t)(d0 >> 2) * S_ + s) * 8);
    u32x2 ya1 = *(const u32x2*)(ws + WS_YA + ((size_t)((d0 >> 2) + 1) * S_ + s) * 8);
    f32x2 st = *(const f32x2*)(ws + WS_STAT + (size_t)s * 8);
    float bias = bf2f(b2[s]);
    unsigned yv[4] = {ya0[0], ya0[1], ya1[0], ya1[1]};
    float r[8];
#pragma unroll
    for (int e = 0; e < 8; ++e) {
      float xe = (e & 1) ? bfhi(xv[e >> 1]) : bflo(xv[e >> 1]);
      float ye = (e & 1) ? bfhi(yv[e >> 1]) : bflo(yv[e >> 1]);
      float xn = rbf(rbf(xe - st[0]) * st[1]);
      r[e] = xe + xn * ye + (acc[nt][e >> 2][e & 3] + bias);
    }
    u32x4 o = {pack_bf16(r[0], r[1]), pack_bf16(r[2], r[3]), pack_bf16(r[4], r[5]), pack_bf16(r[6], r[7])};
    *(u32x4*)(out + (size_t)s * D_ + d0) = o;
  }
  STAMPIT(ws, 3, 2);
}

extern "C" void kernel_launch(void* const* d_in, const int* in_sizes, int n_in, void* d_out, int out_size, void* d_ws,
                              size_t ws_size, hipStream_t stream) {
  const u16* x = (const u16*)d_in[0];
  const u16* k = (const u16*)d_in[1];
  const u16* v = (const u16*)d_in[2];
  const u16* shk = (const u16*)d_in[3];
  const u16* ssmk = (const u16*)d_in[4];
  const u16* ssmD = (const u16*)d_in[5];
  const u16* w1 = (const u16*)d_in[7];
  const u16* b1 = (const u16*)d_in[8];
  const u16* w2 = (const u16*)d_in[9];
  const u16* b2 = (const u16*)d_in[10];
  char* ws = (char*)d_ws;
  (void)hipFuncSetAttribute((const void*)k_gemm1, hipFuncAttributeMaxDynamicSharedMemorySize, G1_LDS);
  (void)hipFuncSetAttribute((const void*)k_gemm2, hipFuncAttributeMaxDynamicSharedMemorySize, G2_LDS);
  k_prologue<<<256, 512, 0, stream>>>(x, k, v, shk, ws);
  k_conv<<<256, 256, CV_LDS, stream>>>(ssmk, ssmD, ws);
  k_gemm1<<<224, 256, G1_LDS, stream>>>(w1, b1, ws);
  k_gemm2<<<256, 256, G2_LDS, stream>>>(w2, b2, x, ws, (u16*)d_out);
}
```

```cpp
#include <hip/hip_runtime.h>
#include <hip/hip_bf16.h>
#include <utility>
#define ABL 0
#define PF2 1
#define WIN1 4
#define WIN2 4
#define G1NS 4
#define G2NS 4
#define PF1 0
#define PF1ROWS 512
#define GPRIO 0
#define POLLSTAGE 24

typedef short    s16x8 __attribute__((ext_vector_type(8)));
typedef short    s16x4 __attribute__((ext_vector_type(4)));
typedef float    f32x4 __attribute__((ext_vector_type(4)));
typedef float    f32x2 __attribute__((ext_vector_type(2)));
typedef unsigned u32x4 __attribute__((ext_vector_type(4)));
typedef unsigned u32x2 __attribute__((ext_vector_type(2)));
typedef unsigned short u16;
#define LDS_AS __attribute__((address_space(3)))
#define DEVINL __device__ __forceinline__
typedef __attribute__((address_space(1))) unsigned gu32;
typedef __attribute__((address_space(1))) unsigned long long gu64;

constexpr int S_ = 2048, D_ = 1024, F_ = 3584;

constexpr size_t WS_HA   = 0;
constexpr size_t WS_KVT  = WS_HA  + (size_t)S_ * D_ * 2;
constexpr size_t WS_YA   = WS_KVT + (size_t)S_ * D_ * 2;
constexpr size_t WS_UA   = WS_YA  + (size_t)S_ * D_ * 2;
constexpr size_t WS_STAT = WS_UA  + (size_t)D_ * F_ * 2;
constexpr size_t WS_FLAG = WS_STAT + (size_t)S_ * 8;

DEVINL float bf2f(unsigned v16) { return __uint_as_float(v16 << 16); }
DEVINL float bflo(unsigned v) { return __uint_as_float(v << 16); }
DEVINL float bfhi(unsigned v) { return __uint_as_float(v & 0xffff0000u); }
DEVINL unsigned pack_bf16(float lo, float hi) {
  typedef __bf16 bf2 __attribute__((ext_vector_type(2)));
  f32x2 v = {lo, hi};
  bf2 r = __builtin_convertvector(v, bf2);
  return __builtin_bit_cast(unsigned, r);
}
DEVINL float rbf(float x) { return bflo(pack_bf16(x, x)); }

template <int N> DEVINL void wait_vm() { asm volatile("s_waitcnt vmcnt(%0)" ::"n"(N) : "memory"); }
template <int I> struct IC { static constexpr int v = I; };
template <int B, int E, typename Fn> DEVINL void static_for(Fn&& f) {
  if constexpr (B < E) { f(IC<B>{}); static_for<B + 1, E>(f); }
}
DEVINL float wave_sum(float v) {
#pragma unroll
  for (int o = 32; o > 0; o >>= 1) v += __shfl_xor(v, o, 64);
  return v;
}
DEVINL s16x4 tr_read(const LDS_AS char* p) {
  return __builtin_amdgcn_ds_read_tr16_b64_v4i16((LDS_AS s16x4*)p);
}
template <int OFF, bool NT> DEVINL void gload16(u32x4& dst, unsigned voff, const char* sbase) {
  if constexpr (NT) asm volatile("global_load_dwordx4 %0, %1, %2 offset:%3 nt" : "=v"(dst) : "v"(voff), "s"(sbase), "n"(OFF));
  else asm volatile("global_load_dwordx4 %0, %1, %2 offset:%3" : "=v"(dst) : "v"(voff), "s"(sbase), "n"(OFF));
}
template <int OFF, bool NT> DEVINL void glds16(unsigned voff, const char* sbase, unsigned ldsaddr) {
  if constexpr (NT) asm volatile("s_mov_b32 m0, %2\n\ts_nop 0\n\tglobal_load_lds_dwordx4 %0, %1 offset:%3 nt" ::"v"(voff), "s"(sbase), "s"(ldsaddr), "n"(OFF) : "memory");
  else asm volatile("s_mov_b32 m0, %2\n\ts_nop 0\n\tglobal_load_lds_dwordx4 %0, %1 offset:%3" ::"v"(voff), "s"(sbase), "s"(ldsaddr), "n"(OFF) : "memory");
}
template <int OFF> DEVINL void glds16_more(unsigned voff, const char* sbase) {
  asm volatile("global_load_lds_dwordx4 %0, %1 offset:%2" ::"v"(voff), "s"(sbase), "n"(OFF) : "memory");
}
DEVINL void mfma_acc(f32x4& c, s16x8 a, s16x8 b) {
  asm volatile("v_mfma_f32_16x16x32_bf16 %0, %1, %2, %0" : "+a"(c) : "v"(a), "v"(b));
}
DEVINL void mfma_accv(f32x4& c, s16x8 a, s16x8 b) {
  asm volatile("v_mfma_f32_16x16x32_bf16 %0, %1, %2, %0" : "+v"(c) : "v"(a), "v"(b));
}
DEVINL void pfload(unsigned& dummy, unsigned voff, const char* sbase) {
  asm volatile("global_load_dword %0, %1, %2" : "+v"(dummy) : "v"(voff), "s"(sbase));
}
DEVINL void sb_arrive(unsigned cnt_addr, int lane) {
  unsigned one = 1u;
  asm volatile("s_mov_b64 exec, 1\n\tds_add_u32 %0, %1\n\ts_mov_b64 exec, -1" ::"v"(cnt_addr), "v"(one) : "memory");
}
DEVINL void sb_poll_issue(unsigned& pv, unsigned cnt_addr) { asm volatile("ds_read_b32 %0, %1" : "=v"(pv) : "v"(cnt_addr) : "memory"); }
template <int YOUNGER> DEVINL void sb_check(unsigned& pv, unsigned cnt_addr, unsigned target) {
  asm volatile("s_waitcnt lgkmcnt(%1)" : "+v"(pv) : "n"(YOUNGER) : "memory");
  unsigned v = __builtin_amdgcn_readfirstlane(pv);
  while (v < target) {
    __builtin_amdgcn_s_sleep(1);
    unsigned t;
    asm volatile("ds_read_b32 %0, %1\n\ts_waitcnt lgkmcnt(0)" : "=v"(t) : "v"(cnt_addr) : "memory");
    v = __builtin_amdgcn_readfirstlane(t);
  }
}
DEVINL void sb_full(unsigned cnt_addr, int lane, unsigned target) {
  asm volatile("s_waitcnt lgkmcnt(0)" ::: "memory");
  sb_arrive(cnt_addr, lane);
  unsigned pv;
  sb_poll_issue(pv, cnt_addr);
  sb_check<0>(pv, cnt_addr, target);
}
DEVINL void gload8(u32x2& dst, unsigned voff, const char* sbase) {
  asm volatile("global_load_dwordx2 %0, %1, %2" : "=v"(dst) : "v"(voff), "s"(sbase));
}
DEVINL void gload2(unsigned& dst, unsigned voff, const char* sbase) {
  asm volatile("global_load_ushort %0, %1, %2" : "=v"(dst) : "v"(voff), "s"(sbase));
}
DEVINL void pin2(u32x4& a, u32x4& b) { asm volatile("" : "+v"(a), "+v"(b)); }
template <typename T> DEVINL T ldnt(const void* p) { return __builtin_nontemporal_load((const T*)p); }
DEVINL float dppf(float v, int ctrl, int rmask, bool bc) { return v; }
template <int CTRL, int RM, bool BC> DEVINL float dpp0(float v) {
  return __builtin_bit_cast(float, __builtin_amdgcn_update_dpp(0, __builtin_bit_cast(int, v), CTRL, RM, 0xf, BC));
}
DEVINL float wave_total(float v) {
  v += dpp0<0x111, 0xf, true>(v); v += dpp0<0x112, 0xf, true>(v); v += dpp0<0x114, 0xf, true>(v); v += dpp0<0x118, 0xf, true>(v);
  v += dpp0<0x142, 0xa, false>(v); v += dpp0<0x143, 0xc, false>(v);
  return __builtin_bit_cast(float, __builtin_amdgcn_readlane(__builtin_bit_cast(int, v), 63));
}
template <bool NT> DEVINL void touch1(unsigned& dummy, unsigned voff, const char* base) {
  if constexpr (NT) asm volatile("global_load_dword %0, %1, %2 nt" : "+v"(dummy) : "v"(voff), "s"(base));
  else asm volatile("global_load_dword %0, %1, %2" : "+v"(dummy) : "v"(voff), "s"(base));
}
template <bool NT> DEVINL void touch_lines(const char* base, unsigned voff, unsigned stride, int n) {
  unsigned dummy = 0u;
  for (int i = 0; i < n; ++i) { touch1<NT>(dummy, voff, base); voff += stride; }
  asm volatile("s_waitcnt vmcnt(0)" : "+v"(dummy)::"memory");
}
DEVINL void pin4(u32x4& a, u32x4& b, u32x4& c, u32x4& d) { asm volatile("" : "+v"(a), "+v"(b), "+v"(c), "+v"(d)); }
DEVINL s16x8 cat8(s16x4 a, s16x4 b) { return __builtin_shufflevector(a, b, 0, 1, 2, 3, 4, 5, 6, 7); }

#define STAMPIT(ws, k, i)
__global__ __launch_bounds__(576) void k_prologue(const u16* __restrict__ x, const u16* __restrict__ kin,
                                                  const u16* __restrict__ vin, const u16* __restrict__ shk,
                                                  const u16* __restrict__ w1, char* __restrict__ ws) {
  __shared__ __attribute__((aligned(16))) u16 sh[8 * 1024];
  const int b = blockIdx.x, tid = threadIdx.x, lane = tid & 63;
  const int w = __builtin_amdgcn_readfirstlane(tid >> 6);
  const int s0 = b * 8;
  STAMPIT(ws, 0, 0);
  if (w == 8) {
    return;
  }
  const u16* xr = x + (size_t)(s0 + w) * D_;
  u32x4 xa = ldnt<u32x4>(xr + lane * 8);
  u32x4 xb = ldnt<u32x4>(xr + 512 + lane * 8);
  const int ks0 = (b >> 3) * 64 + w * 8, kd = (b & 7) * 128 + 2 * lane;
  unsigned kk[11], vv[8];
#pragma unroll
  for (int i = 0; i < 11; ++i) {
    int s = ks0 - 3 + i;
    kk[i] = ldnt<unsigned>(kin + (size_t)(s < 0 ? 0 : s) * D_ + kd);
    if (i < 3 && s < 0) kk[i] = 0u;
  }
#pragma unroll
  for (int i = 0; i < 8; ++i) vv[i] = ldnt<unsigned>(vin + (size_t)(ks0 + i) * D_ + kd);
  u32x4 sk = *(const u32x4*)(shk + (size_t)kd * 4);

  f32x2 f[8];
#pragma unroll
  for (int i = 0; i < 4; ++i) { f[i] = (f32x2){bflo(xa[i]), bfhi(xa[i])}; f[4 + i] = (f32x2){bflo(xb[i]), bfhi(xb[i])}; }
  unsigned hp[8];
#pragma unroll
  for (int pass = 0; pass < 2; ++pass) {
    f32x2 s = f[0];
#pragma unroll
    for (int i = 1; i < 8; ++i) s += f[i];
    const float mean = wave_total(s[0] + s[1]) * (1.0f / D_);
    f32x2 q = {0.f, 0.f};
#pragma unroll
    for (int i = 0; i < 8; ++i) { f32x2 dl = f[i] - mean; q += dl * dl; }
    const float var = wave_total(q[0] + q[1]) * (1.0f / D_);
    const float mb = rbf(mean);
    const float rinv = 1.0f / rbf(sqrtf(rbf(rbf(var) + 1e-5f)));
#pragma unroll
    for (int i = 0; i < 8; ++i) {
      f32x2 t = (f[i] - mb) * rinv;
      hp[i] = pack_bf16(t[0], t[1]);
      if (pass == 0) f[i] = (f32x2){bflo(hp[i]), bfhi(hp[i])};
    }
    if (pass == 0 && lane == 0) { f32x2 st = {mb, rinv}; *(f32x2*)(ws + WS_STAT + (size_t)(s0 + w) * 8) = st; }
  }
  *(u32x4*)(sh + w * 1024 + lane * 8) = (u32x4){hp[0], hp[1], hp[2], hp[3]};
  *(u32x4*)(sh + w * 1024 + 512 + lane * 8) = (u32x4){hp[4], hp[5], hp[6], hp[7]};
  {
    const f32x2 tp[4] = {{bflo(sk[0]), bflo(sk[2])}, {bfhi(sk[0]), bfhi(sk[2])}, {bflo(sk[1]), bflo(sk[3])}, {bfhi(sk[1]), bfhi(sk[3])}};
    f32x2 kf2[11], r[8];
#pragma unroll
    for (int i = 0; i < 11; ++i) kf2[i] = (f32x2){bflo(kk[i]), bfhi(kk[i])};
#pragma unroll
    for (int i = 0; i < 8; ++i) {
      f32x2 a = kf2[i] * tp[3];
      a += kf2[i + 1] * tp[2];
      a += kf2[i + 2] * tp[1];
      a += kf2[i + 3] * tp[0];
      r[i] = a * (f32x2){bflo(vv[i]), bfhi(vv[i])};
    }
    u32x4 o0, o1;
#pragma unroll
    for (int i = 0; i < 4; ++i) { o0[i] = pack_bf16(r[2 * i][0], r[2 * i + 1][0]); o1[i] = pack_bf16(r[2 * i][1], r[2 * i + 1][1]); }
    u32x4* dst = (u32x4*)(ws + WS_KVT + ((size_t)kd * S_ + ks0) * 2);
    dst[0] = o0; dst[S_ / 8] = o1;
  }
  STAMPIT(ws, 0, 1);
  __syncthreads();
  {
    unsigned c[8];
#pragma unroll
    for (int i = 0; i < 8; ++i) c[i] = *(const unsigned*)(sh + i * 1024 + 2 * tid);
    u32x4 o0, o1;
#pragma unroll
    for (int i = 0; i < 4; ++i) {
      o0[i] = __builtin_amdgcn_perm(c[2 * i + 1], c[2 * i], 0x05040100u);
      o1[i] = __builtin_amdgcn_perm(c[2 * i + 1], c[2 * i], 0x07060302u);
    }
    const int d = 2 * tid, dblk = d >> 5, n = (d >> 2) & 1, cc = 4 * ((d >> 3) & 3) + (d & 3);
    const int kstep = b >> 2, g = b & 3;
    u32x4* dst = (u32x4*)(ws + WS_HA) + ((size_t)(dblk * 64 + kstep) * 2 + n) * 64 + cc + 16 * g;
    dst[0] = o0; dst[1] = o1;
  }
  STAMPIT(ws, 0, 2);
}

constexpr int CV_XPOFF = 4160;
constexpr int CV_XP = 1152;
constexpr int CV_WAVE = CV_XPOFF + 4 * CV_XP;
constexpr int CV_LDS = 4 * CV_WAVE;

DEVINL void conv_wave(LDS_AS char* kf, int d, int lane, const u16* __restrict__ ssmk, const u16* __restrict__ ssmD,
                      char* __restrict__ ws, bool bar) {
  LDS_AS char* xp = kf + CV_XPOFF;
  u32x4 kfv[4], kvv[4];
#pragma unroll
  for (int i = 0; i < 4; ++i) kfv[i] = ldnt<u32x4>(ssmk + (size_t)d * S_ + (i * 64 + lane) * 8);
#pragma unroll
  for (int i = 0; i < 4; ++i) kvv[i] = *((const u32x4*)(ws + WS_KVT + (size_t)d * (S_ * 2)) + i * 64 + lane);
  const float Dd = bf2f(ssmD[d]);
  if (lane < 8) *(LDS_AS unsigned*)(kf + lane * 4) = 0u;
  {
    u32x2 z = {0u, 0u};
    *(LDS_AS u32x2*)(xp + (lane >> 4) * CV_XP + (lane & 15) * 8) = z;
  }
  if (bar) { __builtin_amdgcn_s_barrier(); __builtin_amdgcn_s_barrier(); __builtin_amdgcn_s_barrier(); }
#pragma unroll
  for (int i = 0; i < 4; ++i) *(LDS_AS u32x4*)(kf + 32 + (i * 64 + lane) * 16) = kfv[i];
#pragma unroll
  for (int i = 0; i < 4; ++i) {
    const int sb = i * 64 + lane;
    unsigned p0 = __builtin_amdgcn_perm(kvv[i][2], kvv[i][0], 0x05040100u);
    unsigned p1 = __builtin_amdgcn_perm(kvv[i][2], kvv[i][0], 0x07060302u);
    unsigned p2 = __builtin_amdgcn_perm(kvv[i][3], kvv[i][1], 0x05040100u);
    unsigned p3 = __builtin_amdgcn_perm(kvv[i][3], kvv[i][1], 0x07060302u);
    *(LDS_AS unsigned*)(xp + 0 * CV_XP + 128 + sb * 4) = p0;
    *(LDS_AS unsigned*)(xp + 1 * CV_XP + 128 + sb * 4) = p1;
    *(LDS_AS unsigned*)(xp + 2 * CV_XP + 128 + sb * 4) = p2;
    *(LDS_AS unsigned*)(xp + 3 * CV_XP + 128 + sb * 4) = p3;
  }
  __builtin_amdgcn_s_waitcnt(0xC07F);
  __builtin_amdgcn_wave_barrier();
  if (bar) __builtin_amdgcn_s_barrier();
  const int g = lane >> 4, q = (lane >> 2) & 3, p = lane & 3, n = lane & 15;
  s16x8 af[16];
  {
    const LDS_AS char* a0 = kf + 2 * (4 * (8 * g + 7 - q) + 4 + 4 * p);
#pragma unroll
    for (int st = 0; st < 16; ++st) {
      s16x4 lo = tr_read(a0 + st * 256);
      s16x4 hi = tr_read(a0 + st * 256 - 32);
      af[st] = cat8(lo, hi);
    }
  }
  const LDS_AS char* b0 = xp + 2 * (64 + 4 * n - 8 * g - 4);
  const int src = ((lane >= 16) ? (lane - 16) : (lane == 0 ? 63 : lane + 47)) * 4;
  float prev0 = 0.f, prev1 = 0.f, prev2 = 0.f;
  u32x2* yrow = (u32x2*)(ws + WS_YA + (size_t)d * (S_ * 2)) + 4 * n + g;
#pragma unroll
  for (int np = 0; np < 4; ++np) {
    asm volatile("" ::: "memory");
    __builtin_amdgcn_sched_barrier(0);
    f32x4 acc[4][2];
#pragma unroll
    for (int ph = 0; ph < 4; ++ph) { acc[ph][0] = (f32x4){0.f, 0.f, 0.f, 0.f}; acc[ph][1] = (f32x4){0.f, 0.f, 0.f, 0.f}; }
#pragma unroll
    for (int beta = -1; beta <= 4 * np + 2; ++beta) {
#pragma unroll
      for (int ph = 0; ph < 4; ++ph) {
        const LDS_AS char* bp = b0 + ph * CV_XP + beta * 64;
        s16x4 lo = *(const LDS_AS s16x4*)(bp);
        s16x4 hi = *(const LDS_AS s16x4*)(bp + 8);
        s16x8 bf = cat8(lo, hi);
#pragma unroll
        for (int h = 0; h < 2; ++h) {
          const int step = 4 * np + 2 * h - beta;
          if (step >= 0 && step <= 15)
            acc[ph][h] = __builtin_amdgcn_mfma_f32_16x16x32_bf16(af[step], bf, acc[ph][h], 0, 0, 0);
        }
      }
      if (bar && (beta & 1) == 0) __builtin_amdgcn_s_barrier();
    }
#pragma unroll
    for (int h = 0; h < 2; ++h) {
      const int nu = 2 * np + h;
      float c0 = acc[1][h][3] + acc[2][h][2] + acc[3][h][1];
      float c1 = acc[2][h][3] + acc[3][h][2];
      float c2 = acc[3][h][3];
      float p0 = __int_as_float(__builtin_amdgcn_ds_bpermute(src, __float_as_int(c0)));
      float p1 = __int_as_float(__builtin_amdgcn_ds_bpermute(src, __float_as_int(c1)));
      float p2 = __int_as_float(__builtin_amdgcn_ds_bpermute(src, __float_as_int(c2)));
      float a0 = (lane == 0) ? prev0 : p0, a1 = (lane == 0) ? prev1 : p1, a2 = (lane == 0) ? prev2 : p2;
      prev0 = p0; prev1 = p1; prev2 = p2;
      float y[4];
      y[0] = acc[0][h][0] + a0;
      y[1] = acc[0][h][1] + acc[1][h][0] + a1;
      y[2] = acc[0][h][2] + acc[1][h][1] + acc[2][h][0] + a2;
      y[3] = acc[0][h][3] + acc[1][h][2] + acc[2][h][1] + acc[3][h][0];
#pragma unroll
      for (int r = 0; r < 4; ++r) {
        float kvv_ = bf2f(*(const LDS_AS u16*)(xp + r * CV_XP + 128 + 2 * (64 * nu + 4 * n + g)));
        y[r] += kvv_ * Dd;
      }
      u32x2 o = {pack_bf16(y[0], y[1]), pack_bf16(y[2], y[3])};
      yrow[64 * nu] = o;
    }
  }
}

#define SB() __builtin_amdgcn_sched_barrier(0)
template <bool G1, int NS, int NST, int WIN, int NLATE, typename Hook>
DEVINL void gemm_main(Hook&& late_loads  ,
                      LDS_AS char* smem, const char* __restrict__ Wg  , const unsigned wrow  ,
                      const char* __restrict__ Xg  , unsigned xoff0, unsigned xoff1  ,
                      f32x4 (&acc)[G1 ? 8 : 4][4], int w, int lane, int rot  ,
                      unsigned cnt_addr, char* wsdbg) {
  constexpr int NWT = G1 ? 8 : 4;
  constexpr int ROWB = NWT * 32;
  constexpr int STB = 16384;
  constexpr int KROWS = STB / ROWB;
  constexpr int KW = KROWS / 64;
  constexpr int NX = 4 * KW;
  constexpr int OPS = 4 + NX;
  constexpr int NF = 8;
  constexpr int LA = 3;
  static_assert(NST % NS == 0 && NS * NX * 4 + NWT * 16 <= 192, "register budget: in-flight X fragments must never be spilled");
  const int g = lane >> 4, q = (lane >> 2) & 3, p = lane & 3, wk = w >> 1;
  unsigned woff[4];
#pragma unroll
  for (int i = 0; i < 4; ++i) {
    int row, cp, key;
    if (G1) { row = 16 * w + 4 * i + (lane >> 4); cp = lane & 15; key = (row & 3) | (((row >> 3) & 1) << 2); }
    else    { row = 32 * w + 8 * i + (lane >> 3); cp = lane & 7;  key = (row >> 1) & 3; }
    woff[i] = (unsigned)row * wrow + 32 * ((cp >> 1) ^ key) + 16 * (cp & 1) - i * 1024;
  }
  const unsigned ldsw = (unsigned)(size_t)smem + w * 4096;
  unsigned roff[NWT];
#pragma unroll
  for (int m = 0; m < NWT; ++m) {
    const int row = G1 ? (8 * g + q) : (4 * g + q);
    const int key = G1 ? (q | ((g & 1) << 2)) : ((row >> 1) & 3);
    roff[m] = (row + 32 * wk) * ROWB + 32 * (m ^ key) + 8 * p;
  }
  constexpr int RSTEP = G1 ? 4 * ROWB : 16 * ROWB;
  u32x4 xf[NS][NX];
  s16x4 wlo[4], whi[4];

  auto stage_of = [&](int st_lin) {
    int st = st_lin;
    if constexpr (WIN > 1) {
      const int wb = st_lin & ~(WIN - 1);
      const int msk = (NST - wb >= WIN) ? (WIN - 1) : (NST % WIN - 1);
      st = wb + ((st_lin + rot) & msk);
    }
    return st;
  };
  auto issue1 = [&](int st, auto SL, auto OP) {
    constexpr int sl = decltype(SL)::v, op = decltype(OP)::v;
    if constexpr (op == 0) glds16<0, false>(woff[0], Wg + (size_t)st * KROWS * wrow, ldsw + sl * STB);
    else if constexpr (op < 4) glds16_more<op * 1024>(woff[op], Wg + (size_t)st * KROWS * wrow);
    else {
      constexpr int xi = op - 4, ki = xi >> 2, h = (xi >> 1) & 1, n = xi & 1;
      gload16<ki * 4096 + n * 1024 - 4096, false>(xf[sl][xi], h ? xoff1 : xoff0, Xg + (size_t)st * (KW * 4096) + 4096);
    }
  };
  auto readh = [&](auto SL, auto J, auto HALF) {
    constexpr int sl = decltype(SL)::v, j = decltype(J)::v, half = decltype(HALF)::v;
    constexpr int ki = j / NWT, m = j % NWT, r = j & 3;
    const LDS_AS char* a = smem + sl * STB + ki * 64 * ROWB + roff[m] + half * RSTEP;
    if constexpr (half == 0) wlo[r] = tr_read(a); else whi[r] = tr_read(a);
  };
  auto mma1 = [&](auto SL, auto J, auto N) {
    constexpr int sl = decltype(SL)::v, j = decltype(J)::v, n = decltype(N)::v;
    constexpr int ki = j / NWT, m = j % NWT, r = j & 3;
    s16x8 xv = __builtin_bit_cast(s16x8, xf[sl][ki * 4 + n]);
    s16x8 wv = cat8(wlo[r], whi[r]);
    if (G1) mfma_accv(acc[m][n], wv, xv);
    else    mfma_accv(acc[m][n], xv, wv);
  };
  auto pin = [&](auto SL) {
    constexpr int sl = decltype(SL)::v;
    pin4(xf[sl][0], xf[sl][1], xf[sl][2], xf[sl][3]);
    if constexpr (NX == 8) pin4(xf[sl][4], xf[sl][5], xf[sl][6], xf[sl][7]);
  };
  auto stage = [&](auto I, auto ISSUE, auto WAITN, int st_lin, unsigned target, auto EXTRA) {
    constexpr int i = decltype(I)::v, nx = (i + 1) % NS, pv = (i + NS - 1) % NS;
    constexpr int waitn = decltype(WAITN)::v;
    constexpr bool do_issue = decltype(ISSUE)::v;
    unsigned pollv;
    const int st = stage_of(st_lin);
    static_for<0, NF>([&](auto J) {
      constexpr int j = decltype(J)::v, ja = j + LA;
      if constexpr (waitn >= 0 && j == NF - LA) { sb_check<(ABL == 2) ? 0 : 4>(pollv, cnt_addr, target); SB(); }
      mma1(IC<i>{}, J, IC<0>{}); SB();
      if constexpr (waitn >= 0 && j == NF - LA - 2) { sb_poll_issue(pollv, cnt_addr); SB(); }
      if constexpr (ja < NF) readh(IC<i>{}, IC<ja>{}, IC<0>{});
      else if constexpr (waitn >= 0) readh(IC<nx>{}, IC<ja - NF>{}, IC<0>{});
      SB();
      mma1(IC<i>{}, J, IC<1>{}); SB();
      if constexpr (ja < NF) readh(IC<i>{}, IC<ja>{}, IC<1>{});
      else if constexpr (waitn >= 0) readh(IC<nx>{}, IC<ja - NF>{}, IC<1>{});
      SB();
      mma1(IC<i>{}, J, IC<2>{}); SB();
      if constexpr (waitn >= 0 && j == 0) {
        wait_vm<waitn * OPS + decltype(EXTRA)::v>();
        pin(IC<nx>{});
        sb_arrive(cnt_addr, lane);
      }
      if constexpr (do_issue) {
        if constexpr (j >= 1 && j <= 4) issue1(st, IC<pv>{}, IC<4 + (j - 1) * KW>{});
        if constexpr (j >= NF - LA) issue1(st, IC<pv>{}, IC<(j == 5) ? 0 : j - 4>{});
      }
      SB();
      mma1(IC<i>{}, J, IC<3>{}); SB();
      if constexpr (do_issue) {
        if constexpr (KW == 2 && j >= 1 && j <= 4) issue1(st, IC<pv>{}, IC<4 + (j - 1) * KW + 1>{});
        if constexpr (j == NF - LA) issue1(st, IC<pv>{}, IC<1>{});
      }
      SB();
    });
  };

  static_for<0, NS - 1>([&](auto I) {
    const int st = stage_of(decltype(I)::v);
    static_for<0, NX>([&](auto OP) { issue1(st, I, IC<4 + decltype(OP)::v>{}); });
    static_for<0, 4>([&](auto OP) { issue1(st, I, OP); });
  });
  wait_vm<(NS - 2) * OPS>();
  pin(IC<0>{});
  sb_full(cnt_addr, lane, 4);
  STAMPIT(wsdbg, G1 ? 2 : 3, 1);
  SB();
  static_for<0, LA>([&](auto J) { readh(IC<0>{}, J, IC<0>{}); readh(IC<0>{}, J, IC<1>{}); });
  SB();

  for (int tb = 0; tb < NST - NS; tb += NS) {
    static_for<0, NS>([&](auto I) {
      stage(I, IC<1>{}, IC<NS - 3>{}, tb + decltype(I)::v + NS - 1, (unsigned)(tb + decltype(I)::v + 2) * 4, IC<0>{});
    });
  }
  static_for<0, NS>([&](auto I) {
    constexpr int i = decltype(I)::v;
    constexpr int outst = (i == NS - 1) ? -1 : ((i == 0) ? (NS - 3) : (NS - 2 - i));
    stage(I, IC<(i == 0)>{}, IC<outst>{}, NST - 1, (unsigned)(NST - NS + i + 2) * 4, IC<(i == 0) ? 0 : NLATE>{});
    if constexpr (i == 0) { late_loads(); SB(); }
  });
  asm volatile("s_nop 15" ::: "memory");
  SB();
}

template <int NWT>
DEVINL void kpair_reduce(LDS_AS char* smem, f32x4 (&acc)[NWT][4], f32x4 (&fin)[NWT / 2][4], int w, int lane,
                         unsigned cnt_addr, unsigned arrivals  ) {
  constexpr int H = NWT / 2;
  sb_full(cnt_addr, lane, arrivals + 4);
  LDS_AS char* mine = smem + (w * H * 4 * 64 + lane) * 16;
  LDS_AS char* other = smem + ((w ^ 2) * H * 4 * 64 + lane) * 16;
  if (w < 2) {
#pragma unroll
    for (int m = 0; m < H; ++m)
#pragma unroll
      for (int n = 0; n < 4; ++n) { *(LDS_AS f32x4*)(mine + (m * 4 + n) * 1024) = acc[H + m][n]; fin[m][n] = acc[m][n]; }
  } else {
#pragma unroll
    for (int m = 0; m < H; ++m)
#pragma unroll
      for (int n = 0; n < 4; ++n) { *(LDS_AS f32x4*)(mine + (m * 4 + n) * 1024) = acc[m][n]; fin[m][n] = acc[H + m][n]; }
  }
  sb_full(cnt_addr, lane, arrivals + 8);
#pragma unroll
  for (int m = 0; m < H; ++m)
#pragma unroll
    for (int n = 0; n < 4; ++n) fin[m][n] += *(const LDS_AS f32x4*)(other + (m * 4 + n) * 1024);
}

constexpr int G1_NS = G1NS, G2_NS = G2NS;
constexpr int G1_CV = G1_NS * 16384, G1_CNT = G1_CV + CV_LDS, G2_YA = G2_NS * 16384, G2_CNT = G2_YA + 16384;
constexpr int G1_LDS = G1_CNT + 16, G2_LDS = (G2_CNT + 16 > 98304) ? G2_CNT + 16 : 98304  ;

__global__ __launch_bounds__(512, 2) void k_gemm1(const u16* __restrict__ w1, const u16* __restrict__ b1,
                                                  const u16* __restrict__ w2, const u16* __restrict__ ssmk,
                                                  const u16* __restrict__ ssmD, char* __restrict__ ws) {
  extern __shared__ __attribute__((aligned(16))) char dsm[];
  const int b = blockIdx.x, xcd = b & 7, j = b >> 3;
  const int tid = threadIdx.x, lane = tid & 63;
  const int w = __builtin_amdgcn_readfirstlane(tid >> 6);
  const unsigned cnt_addr = (unsigned)(size_t)(LDS_AS char*)dsm + G1_CNT;
  if (tid == 0) *(LDS_AS unsigned*)((LDS_AS char*)dsm + G1_CNT) = 0u;
  __syncthreads();
  if (w >= 4) {
    conv_wave((LDS_AS char*)dsm + G1_CV + (w - 4) * CV_WAVE, (xcd * 32 + j) * 4 + (w - 4), lane, ssmk, ssmD, ws, false);
    return;
  }
  if (j >= 28) {
    const int wv = (b - 224) * 4 + w;
    touch_lines<false>((const char*)w2 + (size_t)wv * 8192, lane * 128, 128 * 8192, 14);
    return;
  }
  const int dt = (xcd & 1) * 4 + (j & 3), ft = (xcd >> 1) * 7 + (j >> 2);
  const int rot = 2 * (j & 3) + (j >> 2);
  const int g = lane >> 4, wk = w >> 1;
  f32x4 acc[8][4];
#pragma unroll
  for (int m = 0; m < 8; ++m)
#pragma unroll
    for (int n = 0; n < 4; ++n) acc[m][n] = (f32x4){0, 0, 0, 0};
  const char* Xg = ws + WS_HA + (size_t)dt * 4 * 64 * 2048;
  const unsigned xoff = (unsigned)((tid >> 6) & 1) * (2 * 64 * 2048) + (unsigned)(tid >> 7) * 2048 + lane * 16;
  u32x2 bb[4];
  auto late = [&]() {
#pragma unroll
    for (int i = 0; i < 4; ++i) gload8(bb[i], (unsigned)(ft * 128 + wk * 64 + 16 * i + 4 * g) * 2, (const char*)b1);
  };
  STAMPIT(ws, 2, 0);
  gemm_main<true, G1_NS, 32, WIN1, 4>(late, (LDS_AS char*)dsm, (const char*)w1 + ft * 256, F_ * 2, Xg, xoff, xoff + 64 * 2048, acc, w,
                                      lane, rot, cnt_addr, ws);
  STAMPIT(ws, 2, 2);
  asm volatile("s_waitcnt vmcnt(0)" : "+v"(bb[0]), "+v"(bb[1]), "+v"(bb[2]), "+v"(bb[3])::"memory");
  f32x4 fin[4][4];
  kpair_reduce<8>((LDS_AS char*)dsm, acc, fin, w, lane, cnt_addr, 32 * 4);
#pragma unroll
  for (int i = 0; i < 2; ++i) {
    float bias[8] = {bflo(bb[2 * i][0]), bfhi(bb[2 * i][0]), bflo(bb[2 * i][1]), bfhi(bb[2 * i][1]),
                     bflo(bb[2 * i + 1][0]), bfhi(bb[2 * i + 1][0]), bflo(bb[2 * i + 1][1]), bfhi(bb[2 * i + 1][1])};
#pragma unroll
    for (int n = 0; n < 4; ++n) {
      float r[8];
#pragma unroll
      for (int e = 0; e < 8; ++e) {
        float t = fin[2 * i + (e >> 2)][n][e & 3] + bias[e];
        float z = t * (-2.3022081985378543f + -0.10294324068f * t * t);
        float ex = __builtin_amdgcn_exp2f(z);
        r[e] = t * __builtin_amdgcn_rcpf(1.0f + ex);
      }
      u32x4 o = {pack_bf16(r[0], r[1]), pack_bf16(r[2], r[3]), pack_bf16(r[4], r[5]), pack_bf16(r[6], r[7])};
      const int dblk = dt * 4 + (w & 1) * 2 + (n >> 1);
      ((u32x4*)(ws + WS_UA))[((size_t)(dblk * 112 + ft * 4 + wk * 2 + i) * 2 + (n & 1)) * 64 + lane] = o;
    }
  }
  STAMPIT(ws, 2, 3);
}

__global__ __launch_bounds__(256, 2) void k_gemm2(const u16* __restrict__ w2, const u16* __restrict__ b2, const u16* __restrict__ x,
                                                  char* __restrict__ ws, u16* __restrict__ out) {
  extern __shared__ __attribute__((aligned(16))) char dsm[];
  const int b = blockIdx.x, xcd = b & 7, j = b >> 3;
  const int dt = (xcd & 1) * 4 + (j & 3), stl = (xcd >> 1) * 8 + (j >> 2);
  const int rot = 2 * (j & 3) + (j >> 2);
  const int tid = threadIdx.x, lane = tid & 63;
  const int w = __builtin_amdgcn_readfirstlane(tid >> 6);
  STAMPIT(ws, 3, 0);
  const unsigned cnt_addr = (unsigned)(size_t)(LDS_AS char*)dsm + G2_CNT;
  if (tid == 0) *(LDS_AS u32x2*)((LDS_AS char*)dsm + G2_CNT) = (u32x2){0u, 0u};
  __syncthreads();
  const int g = lane >> 4, n = lane & 15, q = (lane >> 2) & 3, p = lane & 3, wk = w >> 1;
  const int d0 = dt * 128 + (w & 1) * 64 + 8 * g;
  const int sbase = stl * 64 + wk * 32;
  u32x4 xv[2][2]; u32x2 stv[2]; unsigned bv[2];
  auto late = [&]() {
    {
      const char* ya = ws + WS_YA + (size_t)(dt * 128 + w * 32) * (S_ * 2) + stl * 128;
      const unsigned ldsy = (unsigned)(size_t)(LDS_AS char*)dsm + G2_YA + w * 4096;
      const int r8 = lane >> 3, cp = lane & 7;
      const unsigned off = (unsigned)r8 * (S_ * 2) + 32 * ((cp >> 1) ^ ((r8 >> 1) & 3)) + 16 * (cp & 1);
#pragma unroll
      for (int i = 0; i < 4; ++i) glds16<0, false>(off, ya + (size_t)i * 8 * (S_ * 2), ldsy + i * 1024);
    }
#pragma unroll
    for (int nt = 0; nt < 2; ++nt) {
      const int s = sbase + 16 * nt + n;
      gload16<0, true>(xv[nt][0], (unsigned)(s * D_ + d0) * 2, (const char*)x);
      gload16<64, true>(xv[nt][1], (unsigned)(s * D_ + d0) * 2, (const char*)x);
      gload8(stv[nt], (unsigned)s * 8, ws + WS_STAT);
      gload2(bv[nt], (unsigned)s * 2, (const char*)b2);
    }
  };
  f32x4 acc[4][4];
#pragma unroll
  for (int m = 0; m < 4; ++m)
#pragma unroll
    for (int nn = 0; nn < 4; ++nn) acc[m][nn] = (f32x4){0, 0, 0, 0};
  const char* Xg = ws + WS_UA + (size_t)dt * 4 * 112 * 2048;
  const unsigned xoff = (unsigned)((tid >> 6) & 1) * (2 * 112 * 2048) + (unsigned)(tid >> 7) * 2048 + lane * 16;
  gemm_main<false, G2_NS, 28, WIN2, 12>(late, (LDS_AS char*)dsm, (const char*)w2 + stl * 128, S_ * 2, Xg, xoff, xoff + 112 * 2048,
                                       acc, w, lane, rot, cnt_addr, ws);
  STAMPIT(ws, 3, 2);
  asm volatile("s_waitcnt vmcnt(0)" : "+v"(xv[0][0]), "+v"(xv[0][1]), "+v"(xv[1][0]), "+v"(xv[1][1]), "+v"(stv[0]), "+v"(stv[1]),
               "+v"(bv[0]), "+v"(bv[1])::"memory");
  f32x4 fin[2][4];
  kpair_reduce<4>((LDS_AS char*)dsm, acc, fin, w, lane, cnt_addr, 28 * 4);
  STAMPIT(ws, 3, 3);
#pragma unroll
  for (int nt = 0; nt < 2; ++nt) {
    const int s = sbase + 16 * nt + n;
#pragma unroll
    for (int h = 0; h < 2; ++h) {
      s16x4 yv[2];
#pragma unroll
      for (int r = 0; r < 2; ++r) {
        const int row = 64 * (w & 1) + 32 * h + 8 * g + 4 * r + q;
        yv[r] = tr_read((LDS_AS char*)dsm + G2_YA + row * 128 + 32 * ((2 * wk + nt) ^ ((row >> 1) & 3)) + 8 * p);
      }
      float r[8];
#pragma unroll
      for (int e = 0; e < 8; ++e) {
        float xe = (e & 1) ? bfhi(xv[nt][h][e >> 1]) : bflo(xv[nt][h][e >> 1]);
        float ye = bf2f((unsigned)(u16)yv[e >> 2][e & 3]);
        float xn = rbf((xe - __uint_as_float(stv[nt][0])) * __uint_as_float(stv[nt][1]));
        r[e] = xe + xn * ye + (fin[nt][2 * h + (e >> 2)][e & 3] + bf2f(bv[nt]));
      }
      u32x4 o = {pack_bf16(r[0], r[1]), pack_bf16(r[2], r[3]), pack_bf16(r[4], r[5]), pack_bf16(r[6], r[7])};
      *(u32x4*)(out + (size_t)s * D_ + d0 + 32 * h) = o;
    }
  }
  STAMPIT(ws, 3, 4);
}

extern "C" void kernel_launch(void* const* d_in, const int* in_sizes, int n_in, void* d_out, int out_size, void* d_ws,
                              size_t ws_size, hipStream_t stream) {
  const u16* x = (const u16*)d_in[0];
  const u16* k = (const u16*)d_in[1];
  const u16* v = (const u16*)d_in[2];
  const u16* shk = (const u16*)d_in[3];
  const u16* ssmk = (const u16*)d_in[4];
  const u16* ssmD = (const u16*)d_in[5];
  const u16* w1 = (const u16*)d_in[7];
  const u16* b1 = (const u16*)d_in[8];
  const u16* w2 = (const u16*)d_in[9];
  const u16* b2 = (const u16*)d_in[10];
  char* ws = (char*)d_ws;
  (void)hipFuncSetAttribute((const void*)k_gemm1, hipFuncAttributeMaxDynamicSharedMemorySize, G1_LDS);
  (void)hipFuncSetAttribute((const void*)k_gemm2, hipFuncAttributeMaxDynamicSharedMemorySize, G2_LDS);
  k_prologue<<<256, 576, 0, stream>>>(x, k, v, shk, w1, ws);
  k_gemm1<<<256, 512, G1_LDS, stream>>>(w1, b1, w2, ssmk, ssmD, ws);
  k_gemm2<<<256, 256, G2_LDS, stream>>>(w2, b2, x, ws, (u16*)d_out);
}
```

```cpp
#include <hip/hip_runtime.h>
#include <hip/hip_bf16.h>
#include <utility>
#define ABL 0
#define PF2 1
#define WIN1 8
#define WIN2 4
#define G1NS 4
#define G2NS 4
#define PF1 0
#define PF1ROWS 512
#define GPRIO 0
#define POLLSTAGE 24

typedef short    s16x8 __attribute__((ext_vector_type(8)));
typedef short    s16x4 __attribute__((ext_vector_type(4)));
typedef float    f32x4 __attribute__((ext_vector_type(4)));
typedef float    f32x2 __attribute__((ext_vector_type(2)));
typedef unsigned u32x4 __attribute__((ext_vector_type(4)));
typedef unsigned u32x2 __attribute__((ext_vector_type(2)));
typedef unsigned short u16;
#define LDS_AS __attribute__((address_space(3)))
#define DEVINL __device__ __forceinline__
typedef __attribute__((address_space(1))) unsigned gu32;
typedef __attribute__((address_space(1))) unsigned long long gu64;

constexpr int S_ = 2048, D_ = 1024, F_ = 3584;

constexpr size_t WS_HA   = 0;
constexpr size_t WS_KVT  = WS_HA  + (size_t)S_ * D_ * 2;
constexpr size_t WS_YA   = WS_KVT + (size_t)S_ * D_ * 2;
constexpr size_t WS_UA   = WS_YA  + (size_t)S_ * D_ * 2;
constexpr size_t WS_STAT = WS_UA  + (size_t)D_ * F_ * 2;
constexpr size_t WS_FLAG1 = WS_STAT + (size_t)S_ * 8;
constexpr size_t WS_FLAG2 = WS_FLAG1 + 2048;
constexpr size_t WS_EPOCH = WS_FLAG2 + 2048;

DEVINL float bf2f(unsigned v16) { return __uint_as_float(v16 << 16); }
DEVINL float bflo(unsigned v) { return __uint_as_float(v << 16); }
DEVINL float bfhi(unsigned v) { return __uint_as_float(v & 0xffff0000u); }
DEVINL unsigned pack_bf16(float lo, float hi) {
  typedef __bf16 bf2 __attribute__((ext_vector_type(2)));
  f32x2 v = {lo, hi};
  bf2 r = __builtin_convertvector(v, bf2);
  return __builtin_bit_cast(unsigned, r);
}
DEVINL float rbf(float x) { return bflo(pack_bf16(x, x)); }

template <int N> DEVINL void wait_vm() { asm volatile("s_waitcnt vmcnt(%0)" ::"n"(N) : "memory"); }
template <int I> struct IC { static constexpr int v = I; };
template <int B, int E, typename Fn> DEVINL void static_for(Fn&& f) {
  if constexpr (B < E) { f(IC<B>{}); static_for<B + 1, E>(f); }
}
DEVINL float wave_sum(float v) {
#pragma unroll
  for (int o = 32; o > 0; o >>= 1) v += __shfl_xor(v, o, 64);
  return v;
}
DEVINL s16x4 tr_read(const LDS_AS char* p) {
  return __builtin_amdgcn_ds_read_tr16_b64_v4i16((LDS_AS s16x4*)p);
}
template <int OFF> DEVINL void gload16_sc1(u32x4& dst, unsigned voff, const char* sbase) {
  asm volatile("global_load_dwordx4 %0, %1, %2 offset:%3 sc1" : "=v"(dst) : "v"(voff), "s"(sbase), "n"(OFF));
}
DEVINL void gload8_sc1(u32x2& dst, unsigned voff, const char* sbase) {
  asm volatile("global_load_dwordx2 %0, %1, %2 sc1" : "=v"(dst) : "v"(voff), "s"(sbase));
}
DEVINL void lds_wait_ge(unsigned addr, unsigned target) {
  for (;;) {
    unsigned t;
    asm volatile("ds_read_b32 %0, %1\n\ts_waitcnt lgkmcnt(0)" : "=v"(t) : "v"(addr) : "memory");
    if (__builtin_amdgcn_readfirstlane(t) >= target) break;
    __builtin_amdgcn_s_sleep(2);
  }
}
template <int OFF, bool NT> DEVINL void gload16(u32x4& dst, unsigned voff, const char* sbase) {
  if constexpr (NT) asm volatile("global_load_dwordx4 %0, %1, %2 offset:%3 nt" : "=v"(dst) : "v"(voff), "s"(sbase), "n"(OFF));
  else asm volatile("global_load_dwordx4 %0, %1, %2 offset:%3" : "=v"(dst) : "v"(voff), "s"(sbase), "n"(OFF));
}
template <int OFF, bool NT> DEVINL void glds16(unsigned voff, const char* sbase, unsigned ldsaddr) {
  if constexpr (NT) asm volatile("s_mov_b32 m0, %2\n\ts_nop 0\n\tglobal_load_lds_dwordx4 %0, %1 offset:%3 nt" ::"v"(voff), "s"(sbase), "s"(ldsaddr), "n"(OFF) : "memory");
  else asm volatile("s_mov_b32 m0, %2\n\ts_nop 0\n\tglobal_load_lds_dwordx4 %0, %1 offset:%3" ::"v"(voff), "s"(sbase), "s"(ldsaddr), "n"(OFF) : "memory");
}
template <int OFF> DEVINL void glds16_more(unsigned voff, const char* sbase) {
  asm volatile("global_load_lds_dwordx4 %0, %1 offset:%2" ::"v"(voff), "s"(sbase), "n"(OFF) : "memory");
}
DEVINL void mfma_acc(f32x4& c, s16x8 a, s16x8 b) {
  asm volatile("v_mfma_f32_16x16x32_bf16 %0, %1, %2, %0" : "+a"(c) : "v"(a), "v"(b));
}
DEVINL void mfma_accv(f32x4& c, s16x8 a, s16x8 b) {
  asm volatile("v_mfma_f32_16x16x32_bf16 %0, %1, %2, %0" : "+v"(c) : "v"(a), "v"(b));
}
DEVINL void pfload(unsigned& dummy, unsigned voff, const char* sbase) {
  asm volatile("global_load_dword %0, %1, %2" : "+v"(dummy) : "v"(voff), "s"(sbase));
}
DEVINL void sb_arrive(unsigned cnt_addr, int lane) {
  unsigned one = 1u;
  asm volatile("s_mov_b64 exec, 1\n\tds_add_u32 %0, %1\n\ts_mov_b64 exec, -1" ::"v"(cnt_addr), "v"(one) : "memory");
}
DEVINL void sb_poll_issue(unsigned& pv, unsigned cnt_addr) { asm volatile("ds_read_b32 %0, %1" : "=v"(pv) : "v"(cnt_addr) : "memory"); }
template <int YOUNGER> DEVINL void sb_check(unsigned& pv, unsigned cnt_addr, unsigned target) {
  asm volatile("s_waitcnt lgkmcnt(%1)" : "+v"(pv) : "n"(YOUNGER) : "memory");
  unsigned v = __builtin_amdgcn_readfirstlane(pv);
  while (v < target) {
    __builtin_amdgcn_s_sleep(1);
    unsigned t;
    asm volatile("ds_read_b32 %0, %1\n\ts_waitcnt lgkmcnt(0)" : "=v"(t) : "v"(cnt_addr) : "memory");
    v = __builtin_amdgcn_readfirstlane(t);
  }
}
DEVINL void sb_full(unsigned cnt_addr, int lane, unsigned target) {
  asm volatile("s_waitcnt lgkmcnt(0)" ::: "memory");
  sb_arrive(cnt_addr, lane);
  unsigned pv;
  sb_poll_issue(pv, cnt_addr);
  sb_check<0>(pv, cnt_addr, target);
}
DEVINL void gload8(u32x2& dst, unsigned voff, const char* sbase) {
  asm volatile("global_load_dwordx2 %0, %1, %2" : "=v"(dst) : "v"(voff), "s"(sbase));
}
DEVINL void gload2(unsigned& dst, unsigned voff, const char* sbase) {
  asm volatile("global_load_ushort %0, %1, %2" : "=v"(dst) : "v"(voff), "s"(sbase));
}
DEVINL void pin2(u32x4& a, u32x4& b) { asm volatile("" : "+v"(a), "+v"(b)); }
template <typename T> DEVINL T ldnt(const void* p) { return __builtin_nontemporal_load((const T*)p); }
DEVINL float dppf(float v, int ctrl, int rmask, bool bc) { return v; }
template <int CTRL, int RM, bool BC> DEVINL float dpp0(float v) {
  return __builtin_bit_cast(float, __builtin_amdgcn_update_dpp(0, __builtin_bit_cast(int, v), CTRL, RM, 0xf, BC));
}
DEVINL float wave_total(float v) {
  v += dpp0<0x111, 0xf, true>(v); v += dpp0<0x112, 0xf, true>(v); v += dpp0<0x114, 0xf, true>(v); v += dpp0<0x118, 0xf, true>(v);
  v += dpp0<0x142, 0xa, false>(v); v += dpp0<0x143, 0xc, false>(v);
  return __builtin_bit_cast(float, __builtin_amdgcn_readlane(__builtin_bit_cast(int, v), 63));
}
template <bool NT> DEVINL void touch1(unsigned& dummy, unsigned voff, const char* base) {
  if constexpr (NT) asm volatile("global_load_dword %0, %1, %2 nt" : "+v"(dummy) : "v"(voff), "s"(base));
  else asm volatile("global_load_dword %0, %1, %2" : "+v"(dummy) : "v"(voff), "s"(base));
}
template <bool NT> DEVINL void touch_lines(const char* base, unsigned voff, unsigned stride, int n) {
  unsigned dummy = 0u;
  for (int i = 0; i < n; ++i) { touch1<NT>(dummy, voff, base); voff += stride; }
  asm volatile("s_waitcnt vmcnt(0)" : "+v"(dummy)::"memory");
}
DEVINL void pin4(u32x4& a, u32x4& b, u32x4& c, u32x4& d) { asm volatile("" : "+v"(a), "+v"(b), "+v"(c), "+v"(d)); }
DEVINL s16x8 cat8(s16x4 a, s16x4 b) { return __builtin_shufflevector(a, b, 0, 1, 2, 3, 4, 5, 6, 7); }

#define STAMPIT(ws, k, i)
constexpr int CV_XPOFF = 4160;
constexpr int CV_XP = 1152;
constexpr int CV_WAVE = CV_XPOFF + 4 * CV_XP;
constexpr int CV_LDS = 4 * CV_WAVE;

DEVINL void conv_wave(LDS_AS char* kf, int d, int lane, u32x4 (&kfv)[4]  , float Dd,
                      char* __restrict__ ws) {
  LDS_AS char* xp = kf + CV_XPOFF;
  u32x4 kvv[4];
  {
    auto rs = __builtin_amdgcn_make_buffer_rsrc(ws + WS_KVT + (size_t)d * (S_ * 2), 0, S_ * 2, 0x00020000);
#pragma unroll
    for (int i = 0; i < 4; ++i) kvv[i] = __builtin_bit_cast(u32x4, __builtin_amdgcn_raw_buffer_load_b128(rs, (i * 64 + lane) * 16, 0, 16));
  }
  if (lane < 8) *(LDS_AS unsigned*)(kf + lane * 4) = 0u;
  {
    u32x2 z = {0u, 0u};
    *(LDS_AS u32x2*)(xp + (lane >> 4) * CV_XP + (lane & 15) * 8) = z;
  }
#pragma unroll
  for (int i = 0; i < 4; ++i) *(LDS_AS u32x4*)(kf + 32 + (i * 64 + lane) * 16) = kfv[i];
#pragma unroll
  for (int i = 0; i < 4; ++i) {
    const int sb = i * 64 + lane;
    unsigned p0 = __builtin_amdgcn_perm(kvv[i][2], kvv[i][0], 0x05040100u);
    unsigned p1 = __builtin_amdgcn_perm(kvv[i][2], kvv[i][0], 0x07060302u);
    unsigned p2 = __builtin_amdgcn_perm(kvv[i][3], kvv[i][1], 0x05040100u);
    unsigned p3 = __builtin_amdgcn_perm(kvv[i][3], kvv[i][1], 0x07060302u);
    *(LDS_AS unsigned*)(xp + 0 * CV_XP + 128 + sb * 4) = p0;
    *(LDS_AS unsigned*)(xp + 1 * CV_XP + 128 + sb * 4) = p1;
    *(LDS_AS unsigned*)(xp + 2 * CV_XP + 128 + sb * 4) = p2;
    *(LDS_AS unsigned*)(xp + 3 * CV_XP + 128 + sb * 4) = p3;
  }
  __builtin_amdgcn_s_waitcnt(0xC07F);
  __builtin_amdgcn_wave_barrier();
  const int g = lane >> 4, q = (lane >> 2) & 3, p = lane & 3, n = lane & 15;
  s16x8 af[16];
  {
    const LDS_AS char* a0 = kf + 2 * (4 * (8 * g + 7 - q) + 4 + 4 * p);
#pragma unroll
    for (int st = 0; st < 16; ++st) {
      s16x4 lo = tr_read(a0 + st * 256);
      s16x4 hi = tr_read(a0 + st * 256 - 32);
      af[st] = cat8(lo, hi);
    }
  }
  const LDS_AS char* b0 = xp + 2 * (64 + 4 * n - 8 * g - 4);
  const int src = ((lane >= 16) ? (lane - 16) : (lane == 0 ? 63 : lane + 47)) * 4;
  float prev0 = 0.f, prev1 = 0.f, prev2 = 0.f;
  gu64* yrow = (gu64*)(ws + WS_YA + (size_t)d * (S_ * 2)) + 4 * n + g;
#pragma unroll
  for (int np = 0; np < 4; ++np) {
    asm volatile("" ::: "memory");
    __builtin_amdgcn_sched_barrier(0);
    f32x4 acc[4][2];
#pragma unroll
    for (int ph = 0; ph < 4; ++ph) { acc[ph][0] = (f32x4){0.f, 0.f, 0.f, 0.f}; acc[ph][1] = (f32x4){0.f, 0.f, 0.f, 0.f}; }
#pragma unroll
    for (int beta = -1; beta <= 4 * np + 2; ++beta) {
#pragma unroll
      for (int ph = 0; ph < 4; ++ph) {
        const LDS_AS char* bp = b0 + ph * CV_XP + beta * 64;
        s16x4 lo = *(const LDS_AS s16x4*)(bp);
        s16x4 hi = *(const LDS_AS s16x4*)(bp + 8);
        s16x8 bf = cat8(lo, hi);
#pragma unroll
        for (int h = 0; h < 2; ++h) {
          const int step = 4 * np + 2 * h - beta;
          if (step >= 0 && step <= 15)
            acc[ph][h] = __builtin_amdgcn_mfma_f32_16x16x32_bf16(af[step], bf, acc[ph][h], 0, 0, 0);
        }
      }
    }
#pragma unroll
    for (int h = 0; h < 2; ++h) {
      const int nu = 2 * np + h;
      float c0 = acc[1][h][3] + acc[2][h][2] + acc[3][h][1];
      float c1 = acc[2][h][3] + acc[3][h][2];
      float c2 = acc[3][h][3];
      float p0 = __int_as_float(__builtin_amdgcn_ds_bpermute(src, __float_as_int(c0)));
      float p1 = __int_as_float(__builtin_amdgcn_ds_bpermute(src, __float_as_int(c1)));
      float p2 = __int_as_float(__builtin_amdgcn_ds_bpermute(src, __float_as_int(c2)));
      float a0 = (lane == 0) ? prev0 : p0, a1 = (lane == 0) ? prev1 : p1, a2 = (lane == 0) ? prev2 : p2;
      prev0 = p0; prev1 = p1; prev2 = p2;
      float y[4];
      y[0] = acc[0][h][0] + a0;
      y[1] = acc[0][h][1] + acc[1][h][0] + a1;
      y[2] = acc[0][h][2] + acc[1][h][1] + acc[2][h][0] + a2;
      y[3] = acc[0][h][3] + acc[1][h][2] + acc[2][h][1] + acc[3][h][0];
#pragma unroll
      for (int r = 0; r < 4; ++r) {
        float kvv_ = bf2f(*(const LDS_AS u16*)(xp + r * CV_XP + 128 + 2 * (64 * nu + 4 * n + g)));
        y[r] += kvv_ * Dd;
      }
      const unsigned long long o = (unsigned long long)pack_bf16(y[0], y[1]) | ((unsigned long long)pack_bf16(y[2], y[3]) << 32);
      __hip_atomic_store(yrow + 64 * nu, o, __ATOMIC_RELAXED, __HIP_MEMORY_SCOPE_AGENT);
    }
  }
}

#define SB() __builtin_amdgcn_sched_barrier(0)
template <bool G1, int NS, int NST, int WIN, int NLATE, bool WONLY  , typename Hook>
DEVINL void gemm_main(Hook&& late_loads  ,
                      LDS_AS char* smem, const char* __restrict__ Wg  , const unsigned wrow  ,
                      const char* __restrict__ Xg  , unsigned xoff0, unsigned xoff1  ,
                      f32x4 (&acc)[G1 ? 8 : 4][4], int w, int lane, int rot  ,
                      unsigned cnt_addr, char* wsdbg) {
  constexpr int NWT = G1 ? 8 : 4;
  constexpr int ROWB = NWT * 32;
  constexpr int STB = 16384;
  constexpr int KROWS = STB / ROWB;
  constexpr int KW = KROWS / 64;
  constexpr int NX = 4 * KW;
  constexpr int OPS = 4 + NX;
  constexpr int NF = 8;
  constexpr int LA = 3;
  static_assert(NST % NS == 0 && NS * NX * 4 + NWT * 16 <= 192, "register budget: in-flight X fragments must never be spilled");
  const int g = lane >> 4, q = (lane >> 2) & 3, p = lane & 3, wk = w >> 1;
  unsigned woff[4];
#pragma unroll
  for (int i = 0; i < 4; ++i) {
    int row, cp, key;
    if (G1) { row = 16 * w + 4 * i + (lane >> 4); cp = lane & 15; key = (row & 3) | (((row >> 3) & 1) << 2); }
    else    { row = 32 * w + 8 * i + (lane >> 3); cp = lane & 7;  key = (row >> 1) & 3; }
    woff[i] = (unsigned)row * wrow + 32 * ((cp >> 1) ^ key) + 16 * (cp & 1) - i * 1024;
  }
  const unsigned ldsw = (unsigned)(size_t)smem + w * 4096;
  unsigned roff[NWT];
#pragma unroll
  for (int m = 0; m < NWT; ++m) {
    const int row = G1 ? (8 * g + q) : (4 * g + q);
    const int key = G1 ? (q | ((g & 1) << 2)) : ((row >> 1) & 3);
    roff[m] = (row + 32 * wk) * ROWB + 32 * (m ^ key) + 8 * p;
  }
  constexpr int RSTEP = G1 ? 4 * ROWB : 16 * ROWB;
  u32x4 xf[NS][NX];
  s16x4 wlo[4], whi[4];

  auto stage_of = [&](int st_lin) {
    int st = st_lin;
    if constexpr (WIN > 1) {
      const int wb = st_lin & ~(WIN - 1);
      const int msk = (NST - wb >= WIN) ? (WIN - 1) : (NST % WIN - 1);
      st = wb + ((st_lin + rot) & msk);
    }
    return st;
  };
  auto issue1 = [&](int st, auto SL, auto OP) {
    constexpr int sl = decltype(SL)::v, op = decltype(OP)::v;
    if constexpr (op == 0) glds16<0, false>(woff[0], Wg + (size_t)st * KROWS * wrow, ldsw + sl * STB);
    else if constexpr (op < 4) glds16_more<op * 1024>(woff[op], Wg + (size_t)st * KROWS * wrow);
    else {
      constexpr int xi = op - 4, ki = xi >> 2, h = (xi >> 1) & 1, n = xi & 1;
      gload16_sc1<ki * 4096 + n * 1024 - 4096>(xf[sl][xi], h ? xoff1 : xoff0, Xg + (size_t)st * (KW * 4096) + 4096);
    }
  };
  auto readh = [&](auto SL, auto J, auto HALF) {
    constexpr int sl = decltype(SL)::v, j = decltype(J)::v, half = decltype(HALF)::v;
    constexpr int ki = j / NWT, m = j % NWT, r = j & 3;
    const LDS_AS char* a = smem + sl * STB + ki * 64 * ROWB + roff[m] + half * RSTEP;
    if constexpr (half == 0) wlo[r] = tr_read(a); else whi[r] = tr_read(a);
  };
  auto mma1 = [&](auto SL, auto J, auto N) {
    constexpr int sl = decltype(SL)::v, j = decltype(J)::v, n = decltype(N)::v;
    constexpr int ki = j / NWT, m = j % NWT, r = j & 3;
    s16x8 xv = __builtin_bit_cast(s16x8, xf[sl][ki * 4 + n]);
    s16x8 wv = cat8(wlo[r], whi[r]);
    if (G1) mfma_accv(acc[m][n], wv, xv);
    else    mfma_accv(acc[m][n], xv, wv);
  };
  auto pin = [&](auto SL) {
    constexpr int sl = decltype(SL)::v;
    pin4(xf[sl][0], xf[sl][1], xf[sl][2], xf[sl][3]);
    if constexpr (NX == 8) pin4(xf[sl][4], xf[sl][5], xf[sl][6], xf[sl][7]);
  };
  auto stage = [&](auto I, auto ISSUE, auto WAITN, int st_lin, unsigned target, auto EXTRA) {
    constexpr int i = decltype(I)::v, nx = (i + 1) % NS, pv = (i + NS - 1) % NS;
    constexpr int waitn = decltype(WAITN)::v;
    constexpr bool do_issue = decltype(ISSUE)::v;
    unsigned pollv;
    const int st = stage_of(st_lin);
    static_for<0, NF>([&](auto J) {
      constexpr int j = decltype(J)::v, ja = j + LA;
      if constexpr (waitn >= 0 && j == NF - LA) { sb_check<(ABL == 2) ? 0 : 4>(pollv, cnt_addr, target); SB(); }
      mma1(IC<i>{}, J, IC<0>{}); SB();
      if constexpr (waitn >= 0 && j == NF - LA - 2) { sb_poll_issue(pollv, cnt_addr); SB(); }
      if constexpr (ja < NF) readh(IC<i>{}, IC<ja>{}, IC<0>{});
      else if constexpr (waitn >= 0) readh(IC<nx>{}, IC<ja - NF>{}, IC<0>{});
      SB();
      mma1(IC<i>{}, J, IC<1>{}); SB();
      if constexpr (ja < NF) readh(IC<i>{}, IC<ja>{}, IC<1>{});
      else if constexpr (waitn >= 0) readh(IC<nx>{}, IC<ja - NF>{}, IC<1>{});
      SB();
      mma1(IC<i>{}, J, IC<2>{}); SB();
      if constexpr (waitn >= 0 && j == 0) {
        wait_vm<waitn * OPS + decltype(EXTRA)::v>();
        pin(IC<nx>{});
        sb_arrive(cnt_addr, lane);
      }
      if constexpr (do_issue) {
        if constexpr (j >= 1 && j <= 4) issue1(st, IC<pv>{}, IC<4 + (j - 1) * KW>{});
        if constexpr (j >= NF - LA) issue1(st, IC<pv>{}, IC<(j == 5) ? 0 : j - 4>{});
      }
      SB();
      mma1(IC<i>{}, J, IC<3>{}); SB();
      if constexpr (do_issue) {
        if constexpr (KW == 2 && j >= 1 && j <= 4) issue1(st, IC<pv>{}, IC<4 + (j - 1) * KW + 1>{});
        if constexpr (j == NF - LA) issue1(st, IC<pv>{}, IC<1>{});
      }
      SB();
    });
  };

  if constexpr (WONLY) {
    static_for<0, NS - 1>([&](auto I) {
      const int st = stage_of(decltype(I)::v);
      static_for<0, 4>([&](auto OP) { issue1(st, I, OP); });
    });
    return;
  }
  static_for<0, NS - 1>([&](auto I) {
    const int st = stage_of(decltype(I)::v);
    static_for<0, NX>([&](auto OP) { issue1(st, I, IC<4 + decltype(OP)::v>{}); });
  });
  wait_vm<(NS - 3) * NX>();
  pin(IC<0>{});
  sb_full(cnt_addr, lane, 4);
  STAMPIT(wsdbg, G1 ? 2 : 3, 1);
  SB();
  static_for<0, LA>([&](auto J) { readh(IC<0>{}, J, IC<0>{}); readh(IC<0>{}, J, IC<1>{}); });
  SB();

  for (int tb = 0; tb < NST - NS; tb += NS) {
    static_for<0, NS>([&](auto I) {
      stage(I, IC<1>{}, IC<NS - 3>{}, tb + decltype(I)::v + NS - 1, (unsigned)(tb + decltype(I)::v + 2) * 4, IC<0>{});
    });
  }
  static_for<0, NS>([&](auto I) {
    constexpr int i = decltype(I)::v;
    constexpr int outst = (i == NS - 1) ? -1 : ((i == 0) ? (NS - 3) : (NS - 2 - i));
    constexpr int LATE_AT = G1 ? 0 : 1;
    stage(I, IC<(i == 0)>{}, IC<outst>{}, NST - 1, (unsigned)(NST - NS + i + 2) * 4, IC<(i <= LATE_AT) ? 0 : NLATE>{});
    if constexpr (i == LATE_AT) { late_loads(); SB(); }
  });
  asm volatile("s_nop 15" ::: "memory");
  SB();
}

template <int NWT>
DEVINL void kpair_reduce(LDS_AS char* smem, f32x4 (&acc)[NWT][4], f32x4 (&fin)[NWT / 2][4], int w, int lane,
                         unsigned cnt_addr, unsigned arrivals  ) {
  constexpr int H = NWT / 2;
  sb_full(cnt_addr, lane, arrivals + 4);
  LDS_AS char* mine = smem + (w * H * 4 * 64 + lane) * 16;
  LDS_AS char* other = smem + ((w ^ 2) * H * 4 * 64 + lane) * 16;
  if (w < 2) {
#pragma unroll
    for (int m = 0; m < H; ++m)
#pragma unroll
      for (int n = 0; n < 4; ++n) { *(LDS_AS f32x4*)(mine + (m * 4 + n) * 1024) = acc[H + m][n]; fin[m][n] = acc[m][n]; }
  } else {
#pragma unroll
    for (int m = 0; m < H; ++m)
#pragma unroll
      for (int n = 0; n < 4; ++n) { *(LDS_AS f32x4*)(mine + (m * 4 + n) * 1024) = acc[m][n]; fin[m][n] = acc[H + m][n]; }
  }
  sb_full(cnt_addr, lane, arrivals + 8);
#pragma unroll
  for (int m = 0; m < H; ++m)
#pragma unroll
    for (int n = 0; n < 4; ++n) fin[m][n] += *(const LDS_AS f32x4*)(other + (m * 4 + n) * 1024);
}

constexpr int G1_NS = G1NS, G2_NS = G2NS;
static_assert(G1_NS == 4 && G2_NS == 4, "LDS map below");
constexpr int L_R1 = 0, L_R2 = 65536, L_YA = 131072, L_SY = 147456, L_TOTAL = L_SY + 64;
enum { SY_G1 = 0, SY_G2 = 4, SY_ARR1 = 8, SY_GO1 = 12, SY_ARR2 = 16, SY_GO2 = 20, SY_CONV = 24, SY_SENT1 = 28, SY_TAG = 32 };

DEVINL void poll_flags(const char* flags, unsigned long long tag, unsigned need4, int lane) {
  auto rs = __builtin_amdgcn_make_buffer_rsrc((void*)flags, 0, 2048, 0x00020000);
  const unsigned tlo = (unsigned)tag, thi = (unsigned)(tag >> 32);
  for (unsigned spins = 0; spins < (1u << 20); ++spins) {
    u32x4 a = __builtin_bit_cast(u32x4, __builtin_amdgcn_raw_buffer_load_b128(rs, lane * 32, 0, 16));
    u32x4 c = __builtin_bit_cast(u32x4, __builtin_amdgcn_raw_buffer_load_b128(rs, lane * 32 + 16, 0, 16));
    unsigned have = (a[0] == tlo && a[1] == thi ? 1u : 0u) | (a[2] == tlo && a[3] == thi ? 2u : 0u) |
                    (c[0] == tlo && c[1] == thi ? 4u : 0u) | (c[2] == tlo && c[3] == thi ? 8u : 0u);
    if (__all((have & need4) == need4)) break;
    __builtin_amdgcn_s_sleep(4);
  }
  __builtin_amdgcn_fence(__ATOMIC_ACQUIRE, "wavefront");
}

__global__ __launch_bounds__(512, 2) void k_fused(const u16* __restrict__ x, const u16* __restrict__ kin, const u16* __restrict__ vin,
                                                  const u16* __restrict__ shk, const u16* __restrict__ ssmk, const u16* __restrict__ ssmD,
                                                  const u16* __restrict__ w1, const u16* __restrict__ b1,
                                                  const u16* __restrict__ w2, const u16* __restrict__ b2,
                                                  char* __restrict__ ws, u16* __restrict__ out) {
  extern __shared__ __attribute__((aligned(16))) char dsm[];
  LDS_AS char* sm = (LDS_AS char*)dsm;
  const int b = blockIdx.x, xcd = b & 7, j = b >> 3;
  const int tid = threadIdx.x, lane = tid & 63;
  const int w = __builtin_amdgcn_readfirstlane(tid >> 6);
  const unsigned sy = (unsigned)(size_t)sm + L_SY;
  STAMPIT(ws, 0, 0);
  if (tid < 8) *(LDS_AS unsigned*)(sm + L_SY + tid * 4) = 0u;
  auto wsr = __builtin_amdgcn_make_buffer_rsrc(ws, 0, (unsigned)WS_FLAG1, 0x00020000);

  unsigned long long epoch = 0;
  if (w == 4) epoch = __hip_atomic_load((gu64*)(ws + WS_EPOCH), __ATOMIC_RELAXED, __HIP_MEMORY_SCOPE_AGENT);
  {
    const int s0 = b * 8;
    const u16* xr = x + (size_t)(s0 + w) * D_;
    u32x4 xa = ldnt<u32x4>(xr + lane * 8);
    u32x4 xb = ldnt<u32x4>(xr + 512 + lane * 8);
    const int ks0 = j * 64 + w * 8, kd = xcd * 128 + 2 * lane;
    unsigned kk[11], vv[8];
#pragma unroll
    for (int i = 0; i < 11; ++i) {
      int s = ks0 - 3 + i;
      kk[i] = ldnt<unsigned>(kin + (size_t)(s < 0 ? 0 : s) * D_ + kd);
      if (i < 3 && s < 0) kk[i] = 0u;
    }
#pragma unroll
    for (int i = 0; i < 8; ++i) vv[i] = ldnt<unsigned>(vin + (size_t)(ks0 + i) * D_ + kd);
    u32x4 sk = *(const u32x4*)(shk + (size_t)kd * 4);
    f32x2 f[8];
#pragma unroll
    for (int i = 0; i < 4; ++i) { f[i] = (f32x2){bflo(xa[i]), bfhi(xa[i])}; f[4 + i] = (f32x2){bflo(xb[i]), bfhi(xb[i])}; }
    unsigned hp[8];
#pragma unroll
    for (int pass = 0; pass < 2; ++pass) {
      f32x2 s = f[0];
#pragma unroll
      for (int i = 1; i < 8; ++i) s += f[i];
      const float mean = wave_total(s[0] + s[1]) * (1.0f / D_);
      f32x2 q = {0.f, 0.f};
#pragma unroll
      for (int i = 0; i < 8; ++i) { f32x2 dl = f[i] - mean; q += dl * dl; }
      const float var = wave_total(q[0] + q[1]) * (1.0f / D_);
      const float mb = rbf(mean);
      const float rinv = 1.0f / rbf(sqrtf(rbf(rbf(var) + 1e-5f)));
#pragma unroll
      for (int i = 0; i < 8; ++i) {
        f32x2 t = (f[i] - mb) * rinv;
        hp[i] = pack_bf16(t[0], t[1]);
        if (pass == 0) f[i] = (f32x2){bflo(hp[i]), bfhi(hp[i])};
      }
      if (pass == 0 && lane == 0) {
        const unsigned long long st = (unsigned long long)__float_as_uint(mb) | ((unsigned long long)__float_as_uint(rinv) << 32);
        __hip_atomic_store((gu64*)(ws + WS_STAT + (size_t)(s0 + w) * 8), st, __ATOMIC_RELAXED, __HIP_MEMORY_SCOPE_AGENT);
      }
    }
    LDS_AS char* hs = sm + L_R2;
    LDS_AS char* ks = sm + L_R2 + 16384;
    *(LDS_AS u32x4*)(hs + w * 2048 + lane * 16) = (u32x4){hp[0], hp[1], hp[2], hp[3]};
    *(LDS_AS u32x4*)(hs + w * 2048 + 1024 + lane * 16) = (u32x4){hp[4], hp[5], hp[6], hp[7]};
    {
      const f32x2 tp[4] = {{bflo(sk[0]), bflo(sk[2])}, {bfhi(sk[0]), bfhi(sk[2])}, {bflo(sk[1]), bflo(sk[3])}, {bfhi(sk[1]), bfhi(sk[3])}};
      f32x2 kf2[11], r[8];
#pragma unroll
      for (int i = 0; i < 11; ++i) kf2[i] = (f32x2){bflo(kk[i]), bfhi(kk[i])};
#pragma unroll
      for (int i = 0; i < 8; ++i) {
        f32x2 a = kf2[i] * tp[3];
        a += kf2[i + 1] * tp[2];
        a += kf2[i + 2] * tp[1];
        a += kf2[i + 3] * tp[0];
        r[i] = a * (f32x2){bflo(vv[i]), bfhi(vv[i])};
      }
      u32x4 o0, o1;
#pragma unroll
      for (int i = 0; i < 4; ++i) { o0[i] = pack_bf16(r[2 * i][0], r[2 * i + 1][0]); o1[i] = pack_bf16(r[2 * i][1], r[2 * i + 1][1]); }
      *(LDS_AS u32x4*)(ks + (2 * lane) * 128 + w * 16) = o0;
      *(LDS_AS u32x4*)(ks + (2 * lane + 1) * 128 + w * 16) = o1;
    }
    __syncthreads();
    const int kstep = b >> 2, g = b & 3;
#pragma unroll
    for (int k = 0; k < 2; ++k) {
      const int qq = k * 512 + tid;
      {
        const int dblk = qq >> 5, n = (qq >> 4) & 1, c = qq & 15;
        const int d = 32 * dblk + 8 * (c >> 2) + 4 * n + (c & 3);
        u32x4 o;
#pragma unroll
        for (int i = 0; i < 4; ++i)
          o[i] = (unsigned)*(const LDS_AS u16*)(hs + (2 * i) * 2048 + 2 * d) | ((unsigned)*(const LDS_AS u16*)(hs + (2 * i + 1) * 2048 + 2 * d) << 16);
        __builtin_amdgcn_raw_buffer_store_b128(o, wsr, (unsigned)WS_HA + (((dblk * 64 + kstep) * 2 + n) * 64 + c + 16 * g) * 16, 0, 16);
      }
      {
        u32x4 o = *(const LDS_AS u32x4*)(ks + qq * 16);
        __builtin_amdgcn_raw_buffer_store_b128(o, wsr, (unsigned)WS_KVT + ((xcd * 128 + (qq >> 3)) * S_ + j * 64) * 2 + (qq & 7) * 16, 0, 16);
      }
    }
    asm volatile("s_waitcnt vmcnt(0) lgkmcnt(0)" ::: "memory");
    sb_arrive(sy + SY_ARR1, lane);
  }
  STAMPIT(ws, 0, 1);

  if (w >= 4) {
    const int d = (xcd * 32 + j) * 4 + (w - 4);
    const unsigned long long tag = epoch + 1;
    if (w == 4) {
      if (lane == 0) *(LDS_AS unsigned long long*)(sm + L_SY + SY_TAG) = tag;
      lds_wait_ge(sy + SY_ARR1, 8);
      if (lane == 0) __hip_atomic_store((gu64*)(ws + WS_FLAG1) + b, tag, __ATOMIC_RELAXED, __HIP_MEMORY_SCOPE_AGENT);
      sb_arrive(sy + SY_SENT1, lane);
    } else {
      lds_wait_ge(sy + SY_SENT1, 1);
    }
    u32x4 kfv[4];
#pragma unroll
    for (int i = 0; i < 4; ++i) kfv[i] = ldnt<u32x4>(ssmk + (size_t)d * S_ + (i * 64 + lane) * 8);
    const float Dd = bf2f(ssmD[d]);
    if (w == 4) {
      poll_flags(ws + WS_FLAG1, tag, 15u, lane);
      if (b == 0 && lane == 0) __hip_atomic_store((gu64*)(ws + WS_EPOCH), tag, __ATOMIC_RELAXED, __HIP_MEMORY_SCOPE_AGENT);
      sb_arrive(sy + SY_GO1, lane);
    } else {
      lds_wait_ge(sy + SY_GO1, 1);
    }
    conv_wave(sm + L_R2 + (w - 4) * CV_WAVE, d, lane, kfv, Dd, ws);
    asm volatile("s_waitcnt vmcnt(0) lgkmcnt(0)" ::: "memory");
    sb_arrive(sy + SY_CONV, lane);
    return;
  }

  const int g = lane >> 4, n = lane & 15, q = (lane >> 2) & 3, p = lane & 3, wk = w >> 1;
  const int dt = (xcd & 1) * 4 + (j & 3);
  const int rot = 2 * (j & 3) + (j >> 2);
  const int stl = (xcd >> 1) * 8 + (j >> 2);
  const char* W2g = (const char*)w2 + stl * 128;
  const char* X2g = ws + WS_UA + (size_t)dt * 4 * 112 * 2048;
  if (j < 28) {
    const int ft = (xcd >> 1) * 7 + (j >> 2);
    f32x4 acc[8][4];
#pragma unroll
    for (int m = 0; m < 8; ++m)
#pragma unroll
      for (int nn = 0; nn < 4; ++nn) acc[m][nn] = (f32x4){0, 0, 0, 0};
    const char* Xg = ws + WS_HA + (size_t)dt * 4 * 64 * 2048;
    const unsigned xoff = (unsigned)((tid >> 6) & 1) * (2 * 64 * 2048) + (unsigned)(tid >> 7) * 2048 + lane * 16;
    u32x2 bb[4];
    auto late = [&]() {
#pragma unroll
      for (int i = 0; i < 4; ++i) gload8(bb[i], (unsigned)(ft * 128 + wk * 64 + 16 * i + 4 * g) * 2, (const char*)b1);
    };
    lds_wait_ge(sy + SY_SENT1, 1);
    gemm_main<true, G1_NS, 32, WIN1, 4, true>(late, sm + L_R1, (const char*)w1 + ft * 256, F_ * 2, Xg, xoff, xoff + 64 * 2048, acc, w,
                                              lane, rot, sy + SY_G1, ws);
    lds_wait_ge(sy + SY_GO1, 1);
    STAMPIT(ws, 2, 0);
    gemm_main<true, G1_NS, 32, WIN1, 4, false>(late, sm + L_R1, (const char*)w1 + ft * 256, F_ * 2, Xg, xoff, xoff + 64 * 2048, acc, w,
                                               lane, rot, sy + SY_G1, ws);
    STAMPIT(ws, 2, 2);
    asm volatile("s_waitcnt vmcnt(0)" : "+v"(bb[0]), "+v"(bb[1]), "+v"(bb[2]), "+v"(bb[3])::"memory");
    {
      f32x4 dummy[4][4];
      lds_wait_ge(sy + SY_CONV, 4);
      gemm_main<false, G2_NS, 28, WIN2, 0, true>([] {}, sm + L_R2, W2g, S_ * 2, X2g, 0u, 0u, dummy, w, lane, rot, sy + SY_G2, ws);
    }
    f32x4 fin[4][4];
    kpair_reduce<8>(sm + L_R1, acc, fin, w, lane, sy + SY_G1, 32 * 4);
#pragma unroll
    for (int i = 0; i < 2; ++i) {
      float bias[8] = {bflo(bb[2 * i][0]), bfhi(bb[2 * i][0]), bflo(bb[2 * i][1]), bfhi(bb[2 * i][1]),
                       bflo(bb[2 * i + 1][0]), bfhi(bb[2 * i + 1][0]), bflo(bb[2 * i + 1][1]), bfhi(bb[2 * i + 1][1])};
#pragma unroll
      for (int nn = 0; nn < 4; ++nn) {
        float r[8];
#pragma unroll
        for (int e = 0; e < 8; ++e) {
          float t = fin[2 * i + (e >> 2)][nn][e & 3] + bias[e];
          float z = t * (-2.3022081985378543f + -0.10294324068f * t * t);
          float ex = __builtin_amdgcn_exp2f(z);
          r[e] = t * __builtin_amdgcn_rcpf(1.0f + ex);
        }
        u32x4 o = {pack_bf16(r[0], r[1]), pack_bf16(r[2], r[3]), pack_bf16(r[4], r[5]), pack_bf16(r[6], r[7])};
        const int dblk = dt * 4 + (w & 1) * 2 + (nn >> 1);
        __builtin_amdgcn_raw_buffer_store_b128(o, wsr, (unsigned)WS_UA + (((dblk * 112 + ft * 4 + wk * 2 + i) * 2 + (nn & 1)) * 64 + lane) * 16, 0, 16);
      }
    }
  } else {
    lds_wait_ge(sy + SY_GO1, 1);
    __builtin_amdgcn_s_sleep(32);
    const int wv = (b - 224) * 4 + w;
    touch_lines<false>((const char*)w2 + (size_t)wv * 8192, lane * 128, 128 * 8192, 14);
    f32x4 dummy[4][4];
    lds_wait_ge(sy + SY_CONV, 4);
    gemm_main<false, G2_NS, 28, WIN2, 0, true>([] {}, sm + L_R2, W2g, S_ * 2, X2g, 0u, 0u, dummy, w, lane, rot, sy + SY_G2, ws);
  }
  asm volatile("s_waitcnt vmcnt(0) lgkmcnt(0)" ::: "memory");
  sb_arrive(sy + SY_ARR2, lane);
  STAMPIT(ws, 2, 3);
  if (w == 0) {
    const unsigned long long tag = *(const LDS_AS unsigned long long*)(sm + L_SY + SY_TAG);
    lds_wait_ge(sy + SY_ARR2, 4);
    if (lane == 0) __hip_atomic_store((gu64*)(ws + WS_FLAG2) + b, tag, __ATOMIC_RELAXED, __HIP_MEMORY_SCOPE_AGENT);
    unsigned need = 0;
#pragma unroll
    for (int i = 0; i < 4; ++i) {
      const int pb = 4 * lane + i, px = pb & 7, pj = pb >> 3;
      if ((((px & 1) * 4 + (pj & 3)) == dt && pj < 28) || px == dt) need |= 1u << i;
    }
    poll_flags(ws + WS_FLAG2, tag, need, lane);
    sb_arrive(sy + SY_GO2, lane);
  } else {
    lds_wait_ge(sy + SY_GO2, 1);
  }
  STAMPIT(ws, 3, 0);
  {
    const int d0 = dt * 128 + (w & 1) * 64 + 8 * g;
    const int sbase = stl * 64 + wk * 32;
    u32x4 xv[2][2], yat[4]; u32x2 stv[2]; unsigned bv[2];
    auto late = [&]() {
#pragma unroll
      for (int i = 0; i < 4; ++i)
        gload16_sc1<0>(yat[i], (unsigned)((dt * 128 + w * 32 + i * 8 + (lane >> 3)) * S_ + stl * 64) * 2 + (lane & 7) * 16, ws + WS_YA);
#pragma unroll
      for (int nt = 0; nt < 2; ++nt) {
        const int s = sbase + 16 * nt + n;
        gload16<0, true>(xv[nt][0], (unsigned)(s * D_ + d0) * 2, (const char*)x);
        gload16<64, true>(xv[nt][1], (unsigned)(s * D_ + d0) * 2, (const char*)x);
        gload8_sc1(stv[nt], (unsigned)s * 8, ws + WS_STAT);
        gload2(bv[nt], (unsigned)s * 2, (const char*)b2);
      }
    };
    f32x4 acc[4][4];
#pragma unroll
    for (int m = 0; m < 4; ++m)
#pragma unroll
      for (int nn = 0; nn < 4; ++nn) acc[m][nn] = (f32x4){0, 0, 0, 0};
    const unsigned xoff = (unsigned)((tid >> 6) & 1) * (2 * 112 * 2048) + (unsigned)(tid >> 7) * 2048 + lane * 16;
    gemm_main<false, G2_NS, 28, WIN2, 12, false>(late, sm + L_R2, W2g, S_ * 2, X2g, xoff, xoff + 112 * 2048, acc, w, lane, rot,
                                                 sy + SY_G2, ws);
    STAMPIT(ws, 3, 2);
    asm volatile("s_waitcnt vmcnt(0)" : "+v"(xv[0][0]), "+v"(xv[0][1]), "+v"(xv[1][0]), "+v"(xv[1][1]), "+v"(stv[0]), "+v"(stv[1]),
                 "+v"(bv[0]), "+v"(bv[1]), "+v"(yat[0]), "+v"(yat[1]), "+v"(yat[2]), "+v"(yat[3])::"memory");
#pragma unroll
    for (int i = 0; i < 4; ++i) {
      const int r8 = lane >> 3, cp = lane & 7;
      *(LDS_AS u32x4*)(sm + L_YA + (w * 32 + i * 8 + r8) * 128 + 32 * ((cp >> 1) ^ ((r8 >> 1) & 3)) + 16 * (cp & 1)) = yat[i];
    }
    f32x4 fin[2][4];
    kpair_reduce<4>(sm + L_R2, acc, fin, w, lane, sy + SY_G2, 28 * 4);
    STAMPIT(ws, 3, 3);
#pragma unroll
    for (int nt = 0; nt < 2; ++nt) {
      const int s = sbase + 16 * nt + n;
#pragma unroll
      for (int h = 0; h < 2; ++h) {
        s16x4 yv[2];
#pragma unroll
        for (int r = 0; r < 2; ++r) {
          const int row = 64 * (w & 1) + 32 * h + 8 * g + 4 * r + q;
          yv[r] = tr_read(sm + L_YA + row * 128 + 32 * ((2 * wk + nt) ^ ((row >> 1) & 3)) + 8 * p);
        }
        float r[8];
#pragma unroll
        for (int e = 0; e < 8; ++e) {
          float xe = (e & 1) ? bfhi(xv[nt][h][e >> 1]) : bflo(xv[nt][h][e >> 1]);
          float ye = bf2f((unsigned)(u16)yv[e >> 2][e & 3]);
          float xn = rbf((xe - __uint_as_float(stv[nt][0])) * __uint_as_float(stv[nt][1]));
          r[e] = xe + xn * ye + (fin[nt][2 * h + (e >> 2)][e & 3] + bf2f(bv[nt]));
        }
        u32x4 o = {pack_bf16(r[0], r[1]), pack_bf16(r[2], r[3]), pack_bf16(r[4], r[5]), pack_bf16(r[6], r[7])};
        *(u32x4*)(out + (size_t)s * D_ + d0 + 32 * h) = o;
      }
    }
    STAMPIT(ws, 3, 4);
  }
}

extern "C" void kernel_launch(void* const* d_in, const int* in_sizes, int n_in, void* d_out, int out_size, void* d_ws,
                              size_t ws_size, hipStream_t stream) {
  (void)hipFuncSetAttribute((const void*)k_fused, hipFuncAttributeMaxDynamicSharedMemorySize, L_TOTAL);
  k_fused<<<256, 512, L_TOTAL, stream>>>((const u16*)d_in[0], (const u16*)d_in[1], (const u16*)d_in[2], (const u16*)d_in[3],
                                         (const u16*)d_in[4], (const u16*)d_in[5], (const u16*)d_in[7], (const u16*)d_in[8],
                                         (const u16*)d_in[9], (const u16*)d_in[10], (char*)d_ws, (u16*)d_out);
}
```

```cpp
#include <hip/hip_runtime.h>
#include <hip/hip_bf16.h>
#include <utility>
#define ABL 0
#define PF2 1
#define WIN1 4
#define WIN2 4
#define G1NS 4
#define G2NS 4
#define PF1 0
#define PF1ROWS 512
#define LEAD1 false
#define LEAD2 false
#define ROT2M 1
#define PFA 0
#define PFADELAY 60
#define PFW1N 0
#define PFW2N 14
#define GPRIO 0
#define POLLSTAGE 24

typedef short    s16x8 __attribute__((ext_vector_type(8)));
typedef short    s16x4 __attribute__((ext_vector_type(4)));
typedef float    f32x4 __attribute__((ext_vector_type(4)));
typedef float    f32x2 __attribute__((ext_vector_type(2)));
typedef unsigned u32x4 __attribute__((ext_vector_type(4)));
typedef unsigned u32x2 __attribute__((ext_vector_type(2)));
typedef unsigned short u16;
#define LDS_AS __attribute__((address_space(3)))
#define DEVINL __device__ __forceinline__
typedef __attribute__((address_space(1))) unsigned gu32;
typedef __attribute__((address_space(1))) unsigned long long gu64;

constexpr int S_ = 2048, D_ = 1024, F_ = 3584;

constexpr size_t WS_HA   = 0;
constexpr size_t WS_KVT  = WS_HA  + (size_t)S_ * D_ * 2;
constexpr size_t WS_YA   = WS_KVT + (size_t)S_ * D_ * 2;
constexpr size_t WS_UA   = WS_YA  + (size_t)S_ * D_ * 2;
constexpr size_t WS_STAT = WS_UA  + (size_t)D_ * F_ * 2;
constexpr size_t WS_FLAG = WS_STAT + (size_t)S_ * 8;

DEVINL float bf2f(unsigned v16) { return __uint_as_float(v16 << 16); }
DEVINL float bflo(unsigned v) { return __uint_as_float(v << 16); }
DEVINL float bfhi(unsigned v) { return __uint_as_float(v & 0xffff0000u); }
DEVINL unsigned pack_bf16(float lo, float hi) {
  typedef __bf16 bf2 __attribute__((ext_vector_type(2)));
  f32x2 v = {lo, hi};
  bf2 r = __builtin_convertvector(v, bf2);
  return __builtin_bit_cast(unsigned, r);
}
DEVINL float rbf(float x) { return bflo(pack_bf16(x, x)); }

template <int N> DEVINL void wait_vm() { asm volatile("s_waitcnt vmcnt(%0)" ::"n"(N) : "memory"); }
template <int I> struct IC { static constexpr int v = I; };
template <int B, int E, typename Fn> DEVINL void static_for(Fn&& f) {
  if constexpr (B < E) { f(IC<B>{}); static_for<B + 1, E>(f); }
}
DEVINL float wave_sum(float v) {
#pragma unroll
  for (int o = 32; o > 0; o >>= 1) v += __shfl_xor(v, o, 64);
  return v;
}
DEVINL s16x4 tr_read(const LDS_AS char* p) {
  return __builtin_amdgcn_ds_read_tr16_b64_v4i16((LDS_AS s16x4*)p);
}
template <int OFF, bool NT> DEVINL void gload16(u32x4& dst, unsigned voff, const char* sbase) {
  if constexpr (NT) asm volatile("global_load_dwordx4 %0, %1, %2 offset:%3 nt" : "=v"(dst) : "v"(voff), "s"(sbase), "n"(OFF));
  else asm volatile("global_load_dwordx4 %0, %1, %2 offset:%3" : "=v"(dst) : "v"(voff), "s"(sbase), "n"(OFF));
}
template <int OFF, bool NT> DEVINL void glds16(unsigned voff, const char* sbase, unsigned ldsaddr) {
  if constexpr (NT) asm volatile("s_mov_b32 m0, %2\n\ts_nop 0\n\tglobal_load_lds_dwordx4 %0, %1 offset:%3 nt" ::"v"(voff), "s"(sbase), "s"(ldsaddr), "n"(OFF) : "memory");
  else asm volatile("s_mov_b32 m0, %2\n\ts_nop 0\n\tglobal_load_lds_dwordx4 %0, %1 offset:%3" ::"v"(voff), "s"(sbase), "s"(ldsaddr), "n"(OFF) : "memory");
}
template <int OFF, bool NT> DEVINL void glds16_more(unsigned voff, const char* sbase) {
  if constexpr (NT) asm volatile("global_load_lds_dwordx4 %0, %1 offset:%2 nt" ::"v"(voff), "s"(sbase), "n"(OFF) : "memory");
  else asm volatile("global_load_lds_dwordx4 %0, %1 offset:%2" ::"v"(voff), "s"(sbase), "n"(OFF) : "memory");
}
DEVINL void mfma_acc(f32x4& c, s16x8 a, s16x8 b) {
  asm volatile("v_mfma_f32_16x16x32_bf16 %0, %1, %2, %0" : "+a"(c) : "v"(a), "v"(b));
}
DEVINL void mfma_accv(f32x4& c, s16x8 a, s16x8 b) {
  asm volatile("v_mfma_f32_16x16x32_bf16 %0, %1, %2, %0" : "+v"(c) : "v"(a), "v"(b));
}
DEVINL void pfload(unsigned& dummy, unsigned voff, const char* sbase) {
  asm volatile("global_load_dword %0, %1, %2" : "+v"(dummy) : "v"(voff), "s"(sbase));
}
DEVINL void sb_arrive(unsigned cnt_addr, int lane) {
  unsigned one = 1u;
  asm volatile("s_mov_b64 exec, 1\n\tds_add_u32 %0, %1\n\ts_mov_b64 exec, -1" ::"v"(cnt_addr), "v"(one) : "memory");
}
DEVINL void sb_poll_issue(unsigned& pv, unsigned cnt_addr) { asm volatile("ds_read_b32 %0, %1" : "=v"(pv) : "v"(cnt_addr) : "memory"); }
template <int YOUNGER> DEVINL void sb_check(unsigned& pv, unsigned cnt_addr, unsigned target) {
  asm volatile("s_waitcnt lgkmcnt(%1)" : "+v"(pv) : "n"(YOUNGER) : "memory");
  unsigned v = __builtin_amdgcn_readfirstlane(pv);
  while (v < target) {
    __builtin_amdgcn_s_sleep(1);
    unsigned t;
    asm volatile("ds_read_b32 %0, %1\n\ts_waitcnt lgkmcnt(0)" : "=v"(t) : "v"(cnt_addr) : "memory");
    v = __builtin_amdgcn_readfirstlane(t);
  }
}
DEVINL void sb_full(unsigned cnt_addr, int lane, unsigned target) {
  asm volatile("s_waitcnt lgkmcnt(0)" ::: "memory");
  sb_arrive(cnt_addr, lane);
  unsigned pv;
  sb_poll_issue(pv, cnt_addr);
  sb_check<0>(pv, cnt_addr, target);
}
DEVINL void gload8(u32x2& dst, unsigned voff, const char* sbase) {
  asm volatile("global_load_dwordx2 %0, %1, %2" : "=v"(dst) : "v"(voff), "s"(sbase));
}
DEVINL void gload2(unsigned& dst, unsigned voff, const char* sbase) {
  asm volatile("global_load_ushort %0, %1, %2" : "=v"(dst) : "v"(voff), "s"(sbase));
}
DEVINL void pin2(u32x4& a, u32x4& b) { asm volatile("" : "+v"(a), "+v"(b)); }
template <typename T> DEVINL T ldnt(const void* p) { return __builtin_nontemporal_load((const T*)p); }
DEVINL float dppf(float v, int ctrl, int rmask, bool bc) { return v; }
template <int CTRL, int RM, bool BC> DEVINL float dpp0(float v) {
  return __builtin_bit_cast(float, __builtin_amdgcn_update_dpp(0, __builtin_bit_cast(int, v), CTRL, RM, 0xf, BC));
}
DEVINL float wave_total(float v) {
  v += dpp0<0x111, 0xf, true>(v); v += dpp0<0x112, 0xf, true>(v); v += dpp0<0x114, 0xf, true>(v); v += dpp0<0x118, 0xf, true>(v);
  v += dpp0<0x142, 0xa, false>(v); v += dpp0<0x143, 0xc, false>(v);
  return __builtin_bit_cast(float, __builtin_amdgcn_readlane(__builtin_bit_cast(int, v), 63));
}
template <bool NT> DEVINL void touch1(unsigned& dummy, unsigned voff, const char* base) {
  if constexpr (NT) asm volatile("global_load_dword %0, %1, %2 nt" : "+v"(dummy) : "v"(voff), "s"(base));
  else asm volatile("global_load_dword %0, %1, %2" : "+v"(dummy) : "v"(voff), "s"(base));
}
template <bool NT> DEVINL void touch_lines(const char* base, unsigned voff, unsigned stride, int n) {
  unsigned dummy = 0u;
  for (int i = 0; i < n; ++i) { touch1<NT>(dummy, voff, base); voff += stride; }
  asm volatile("s_waitcnt vmcnt(0)" : "+v"(dummy)::"memory");
}
DEVINL void pin4(u32x4& a, u32x4& b, u32x4& c, u32x4& d) { asm volatile("" : "+v"(a), "+v"(b), "+v"(c), "+v"(d)); }
DEVINL s16x8 cat8(s16x4 a, s16x4 b) { return __builtin_shufflevector(a, b, 0, 1, 2, 3, 4, 5, 6, 7); }

#define STAMPIT(ws, k, i)
__global__ __launch_bounds__(576) void k_prologue(const u16* __restrict__ x, const u16* __restrict__ kin,
                                                  const u16* __restrict__ vin, const u16* __restrict__ shk,
                                                  const u16* __restrict__ w1, char* __restrict__ ws) {
  __shared__ __attribute__((aligned(16))) u16 sh[8 * 1024];
  const int b = blockIdx.x, tid = threadIdx.x, lane = tid & 63;
  const int w = __builtin_amdgcn_readfirstlane(tid >> 6);
  const int s0 = b * 8;
  STAMPIT(ws, 0, 0);
  if (w == 8) {
    return;
  }
  const u16* xr = x + (size_t)(s0 + w) * D_;
  u32x4 xa = ldnt<u32x4>(xr + lane * 8);
  u32x4 xb = ldnt<u32x4>(xr + 512 + lane * 8);
  const int ks0 = (b >> 3) * 64 + w * 8, kd = (b & 7) * 128 + 2 * lane;
  unsigned kk[11], vv[8];
#pragma unroll
  for (int i = 0; i < 11; ++i) {
    int s = ks0 - 3 + i;
    kk[i] = ldnt<unsigned>(kin + (size_t)(s < 0 ? 0 : s) * D_ + kd);
    if (i < 3 && s < 0) kk[i] = 0u;
  }
#pragma unroll
  for (int i = 0; i < 8; ++i) vv[i] = ldnt<unsigned>(vin + (size_t)(ks0 + i) * D_ + kd);
  u32x4 sk = *(const u32x4*)(shk + (size_t)kd * 4);

  f32x2 f[8];
#pragma unroll
  for (int i = 0; i < 4; ++i) { f[i] = (f32x2){bflo(xa[i]), bfhi(xa[i])}; f[4 + i] = (f32x2){bflo(xb[i]), bfhi(xb[i])}; }
  unsigned hp[8];
#pragma unroll
  for (int pass = 0; pass < 2; ++pass) {
    f32x2 s = f[0];
#pragma unroll
    for (int i = 1; i < 8; ++i) s += f[i];
    const float mean = wave_total(s[0] + s[1]) * (1.0f / D_);
    f32x2 q = {0.f, 0.f};
#pragma unroll
    for (int i = 0; i < 8; ++i) { f32x2 dl = f[i] - mean; q += dl * dl; }
    const float var = wave_total(q[0] + q[1]) * (1.0f / D_);
    const float mb = rbf(mean);
    const float rinv = 1.0f / rbf(sqrtf(rbf(rbf(var) + 1e-5f)));
#pragma unroll
    for (int i = 0; i < 8; ++i) {
      f32x2 t = (f[i] - mb) * rinv;
      hp[i] = pack_bf16(t[0], t[1]);
      if (pass == 0) f[i] = (f32x2){bflo(hp[i]), bfhi(hp[i])};
    }
    if (pass == 0 && lane == 0) { f32x2 st = {mb, rinv}; *(f32x2*)(ws + WS_STAT + (size_t)(s0 + w) * 8) = st; }
  }
  *(u32x4*)(sh + w * 1024 + lane * 8) = (u32x4){hp[0], hp[1], hp[2], hp[3]};
  *(u32x4*)(sh + w * 1024 + 512 + lane * 8) = (u32x4){hp[4], hp[5], hp[6], hp[7]};
  {
    const f32x2 tp[4] = {{bflo(sk[0]), bflo(sk[2])}, {bfhi(sk[0]), bfhi(sk[2])}, {bflo(sk[1]), bflo(sk[3])}, {bfhi(sk[1]), bfhi(sk[3])}};
    f32x2 kf2[11], r[8];
#pragma unroll
    for (int i = 0; i < 11; ++i) kf2[i] = (f32x2){bflo(kk[i]), bfhi(kk[i])};
#pragma unroll
    for (int i = 0; i < 8; ++i) {
      f32x2 a = kf2[i] * tp[3];
      a += kf2[i + 1] * tp[2];
      a += kf2[i + 2] * tp[1];
      a += kf2[i + 3] * tp[0];
      r[i] = a * (f32x2){bflo(vv[i]), bfhi(vv[i])};
    }
    u32x4 o0, o1;
#pragma unroll
    for (int i = 0; i < 4; ++i) { o0[i] = pack_bf16(r[2 * i][0], r[2 * i + 1][0]); o1[i] = pack_bf16(r[2 * i][1], r[2 * i + 1][1]); }
    u32x4* dst = (u32x4*)(ws + WS_KVT + ((size_t)kd * S_ + ks0) * 2);
    dst[0] = o0; dst[S_ / 8] = o1;
  }
  STAMPIT(ws, 0, 1);
  __syncthreads();
  {
    unsigned c[8];
#pragma unroll
    for (int i = 0; i < 8; ++i) c[i] = *(const unsigned*)(sh + i * 1024 + 2 * tid);
    u32x4 o0, o1;
#pragma unroll
    for (int i = 0; i < 4; ++i) {
      o0[i] = __builtin_amdgcn_perm(c[2 * i + 1], c[2 * i], 0x05040100u);
      o1[i] = __builtin_amdgcn_perm(c[2 * i + 1], c[2 * i], 0x07060302u);
    }
    const int d = 2 * tid, dblk = d >> 5, n = (d >> 2) & 1, cc = 4 * ((d >> 3) & 3) + (d & 3);
    const int kstep = b >> 2, g = b & 3;
    u32x4* dst = (u32x4*)(ws + WS_HA) + ((size_t)(dblk * 64 + kstep) * 2 + n) * 64 + cc + 16 * g;
    dst[0] = o0; dst[1] = o1;
  }
  STAMPIT(ws, 0, 2);
}

constexpr int CV_XPOFF = 4160;
constexpr int CV_XP = 1152;
constexpr int CV_WAVE = CV_XPOFF + 4 * CV_XP;
constexpr int CV_LDS = 4 * CV_WAVE;

DEVINL void conv_wave(LDS_AS char* kf, int d, int lane, const u16* __restrict__ ssmk, const u16* __restrict__ ssmD,
                      char* __restrict__ ws, bool bar) {
  LDS_AS char* xp = kf + CV_XPOFF;
  u32x4 kfv[4], kvv[4];
#pragma unroll
  for (int i = 0; i < 4; ++i) kfv[i] = ldnt<u32x4>(ssmk + (size_t)d * S_ + (i * 64 + lane) * 8);
#pragma unroll
  for (int i = 0; i < 4; ++i) kvv[i] = *((const u32x4*)(ws + WS_KVT + (size_t)d * (S_ * 2)) + i * 64 + lane);
  const float Dd = bf2f(ssmD[d]);
  if (lane < 8) *(LDS_AS unsigned*)(kf + lane * 4) = 0u;
  {
    u32x2 z = {0u, 0u};
    *(LDS_AS u32x2*)(xp + (lane >> 4) * CV_XP + (lane & 15) * 8) = z;
  }
  if (bar) { __builtin_amdgcn_s_barrier(); __builtin_amdgcn_s_barrier(); __builtin_amdgcn_s_barrier(); }
#pragma unroll
  for (int i = 0; i < 4; ++i) *(LDS_AS u32x4*)(kf + 32 + (i * 64 + lane) * 16) = kfv[i];
#pragma unroll
  for (int i = 0; i < 4; ++i) {
    const int sb = i * 64 + lane;
    unsigned p0 = __builtin_amdgcn_perm(kvv[i][2], kvv[i][0], 0x05040100u);
    unsigned p1 = __builtin_amdgcn_perm(kvv[i][2], kvv[i][0], 0x07060302u);
    unsigned p2 = __builtin_amdgcn_perm(kvv[i][3], kvv[i][1], 0x05040100u);
    unsigned p3 = __builtin_amdgcn_perm(kvv[i][3], kvv[i][1], 0x07060302u);
    *(LDS_AS unsigned*)(xp + 0 * CV_XP + 128 + sb * 4) = p0;
    *(LDS_AS unsigned*)(xp + 1 * CV_XP + 128 + sb * 4) = p1;
    *(LDS_AS unsigned*)(xp + 2 * CV_XP + 128 + sb * 4) = p2;
    *(LDS_AS unsigned*)(xp + 3 * CV_XP + 128 + sb * 4) = p3;
  }
  __builtin_amdgcn_s_waitcnt(0xC07F);
  __builtin_amdgcn_wave_barrier();
  if (bar) __builtin_amdgcn_s_barrier();
  const int g = lane >> 4, q = (lane >> 2) & 3, p = lane & 3, n = lane & 15;
  s16x8 af[16];
  {
    const LDS_AS char* a0 = kf + 2 * (4 * (8 * g + 7 - q) + 4 + 4 * p);
#pragma unroll
    for (int st = 0; st < 16; ++st) {
      s16x4 lo = tr_read(a0 + st * 256);
      s16x4 hi = tr_read(a0 + st * 256 - 32);
      af[st] = cat8(lo, hi);
    }
  }
  const LDS_AS char* b0 = xp + 2 * (64 + 4 * n - 8 * g - 4);
  const int src = ((lane >= 16) ? (lane - 16) : (lane == 0 ? 63 : lane + 47)) * 4;
  float prev0 = 0.f, prev1 = 0.f, prev2 = 0.f;
  u32x2* yrow = (u32x2*)(ws + WS_YA + (size_t)d * (S_ * 2)) + 4 * n + g;
#pragma unroll
  for (int np = 0; np < 4; ++np) {
    asm volatile("" ::: "memory");
    __builtin_amdgcn_sched_barrier(0);
    f32x4 acc[4][2];
#pragma unroll
    for (int ph = 0; ph < 4; ++ph) { acc[ph][0] = (f32x4){0.f, 0.f, 0.f, 0.f}; acc[ph][1] = (f32x4){0.f, 0.f, 0.f, 0.f}; }
#pragma unroll
    for (int beta = -1; beta <= 4 * np + 2; ++beta) {
#pragma unroll
      for (int ph = 0; ph < 4; ++ph) {
        const LDS_AS char* bp = b0 + ph * CV_XP + beta * 64;
        s16x4 lo = *(const LDS_AS s16x4*)(bp);
        s16x4 hi = *(const LDS_AS s16x4*)(bp + 8);
        s16x8 bf = cat8(lo, hi);
#pragma unroll
        for (int h = 0; h < 2; ++h) {
          const int step = 4 * np + 2 * h - beta;
          if (step >= 0 && step <= 15)
            acc[ph][h] = __builtin_amdgcn_mfma_f32_16x16x32_bf16(af[step], bf, acc[ph][h], 0, 0, 0);
        }
      }
      if (bar && (beta & 1) == 0) __builtin_amdgcn_s_barrier();
    }
#pragma unroll
    for (int h = 0; h < 2; ++h) {
      const int nu = 2 * np + h;
      float c0 = acc[1][h][3] + acc[2][h][2] + acc[3][h][1];
      float c1 = acc[2][h][3] + acc[3][h][2];
      float c2 = acc[3][h][3];
      float p0 = __int_as_float(__builtin_amdgcn_ds_bpermute(src, __float_as_int(c0)));
      float p1 = __int_as_float(__builtin_amdgcn_ds_bpermute(src, __float_as_int(c1)));
      float p2 = __int_as_float(__builtin_amdgcn_ds_bpermute(src, __float_as_int(c2)));
      float a0 = (lane == 0) ? prev0 : p0, a1 = (lane == 0) ? prev1 : p1, a2 = (lane == 0) ? prev2 : p2;
      prev0 = p0; prev1 = p1; prev2 = p2;
      float y[4];
      y[0] = acc[0][h][0] + a0;
      y[1] = acc[0][h][1] + acc[1][h][0] + a1;
      y[2] = acc[0][h][2] + acc[1][h][1] + acc[2][h][0] + a2;
      y[3] = acc[0][h][3] + acc[1][h][2] + acc[2][h][1] + acc[3][h][0];
#pragma unroll
      for (int r = 0; r < 4; ++r) {
        float kvv_ = bf2f(*(const LDS_AS u16*)(xp + r * CV_XP + 128 + 2 * (64 * nu + 4 * n + g)));
        y[r] += kvv_ * Dd;
      }
      u32x2 o = {pack_bf16(y[0], y[1]), pack_bf16(y[2], y[3])};
      yrow[64 * nu] = o;
    }
  }
}

#define SB() __builtin_amdgcn_sched_barrier(0)
template <bool G1, int NS, int NST, int WIN, int NLATE, typename Hook>
DEVINL void gemm_main(Hook&& late_loads  ,
                      LDS_AS char* smem, const char* __restrict__ Wg  , const unsigned wrow  ,
                      const char* __restrict__ Xg  , unsigned xoff0, unsigned xoff1  ,
                      f32x4 (&acc)[G1 ? 8 : 4][4], int w, int lane, int rot  ,
                      unsigned cnt_addr, char* wsdbg) {
  constexpr int NWT = G1 ? 8 : 4;
  constexpr int ROWB = NWT * 32;
  constexpr int STB = 16384;
  constexpr int KROWS = STB / ROWB;
  constexpr int KW = KROWS / 64;
  constexpr int NX = 4 * KW;
  constexpr int OPS = 4 + NX;
  constexpr int NF = 8;
  constexpr int LA = 3;
  static_assert(NST % NS == 0 && NS * NX * 4 + NWT * 16 <= 192, "register budget: in-flight X fragments must never be spilled");
  const int g = lane >> 4, q = (lane >> 2) & 3, p = lane & 3, wk = w >> 1;
  unsigned woff[4];
#pragma unroll
  for (int i = 0; i < 4; ++i) {
    int row, cp, key;
    if (G1) { row = 16 * w + 4 * i + (lane >> 4); cp = lane & 15; key = (row & 3) | (((row >> 3) & 1) << 2); }
    else    { row = 32 * w + 8 * i + (lane >> 3); cp = lane & 7;  key = (row >> 1) & 3; }
    woff[i] = (unsigned)row * wrow + 32 * ((cp >> 1) ^ key) + 16 * (cp & 1) - i * 1024;
  }
  const unsigned ldsw = (unsigned)(size_t)smem + w * 4096;
  unsigned roff[NWT];
#pragma unroll
  for (int m = 0; m < NWT; ++m) {
    const int row = G1 ? (8 * g + q) : (4 * g + q);
    const int key = G1 ? (q | ((g & 1) << 2)) : ((row >> 1) & 3);
    roff[m] = (row + 32 * wk) * ROWB + 32 * (m ^ key) + 8 * p;
  }
  constexpr int RSTEP = G1 ? 4 * ROWB : 16 * ROWB;
  u32x4 xf[NS][NX];
  s16x4 wlo[4], whi[4];

  auto stage_of = [&](int st_lin) {
    int st = st_lin;
    if constexpr (WIN > 1) {
      const int wb = st_lin & ~(WIN - 1);
      const int msk = (NST - wb >= WIN) ? (WIN - 1) : (NST % WIN - 1);
      st = wb + ((st_lin + rot) & msk);
    }
    return st;
  };
  auto issue1 = [&](int st, auto SL, auto OP) {
    constexpr int sl = decltype(SL)::v, op = decltype(OP)::v;
    constexpr bool lead = (G1 ? LEAD1 : LEAD2) && WIN == NS && sl == 0;
    if constexpr (op == 0) glds16<0, lead>(woff[0], Wg + (size_t)st * KROWS * wrow, ldsw + sl * STB);
    else if constexpr (op < 4) glds16_more<op * 1024, lead>(woff[op], Wg + (size_t)st * KROWS * wrow);
    else {
      constexpr int xi = op - 4, ki = xi >> 2, h = (xi >> 1) & 1, n = xi & 1;
      gload16<ki * 4096 + n * 1024 - 4096, false>(xf[sl][xi], h ? xoff1 : xoff0, Xg + (size_t)st * (KW * 4096) + 4096);
    }
  };
  auto readh = [&](auto SL, auto J, auto HALF) {
    constexpr int sl = decltype(SL)::v, j = decltype(J)::v, half = decltype(HALF)::v;
    constexpr int ki = j / NWT, m = j % NWT, r = j & 3;
    const LDS_AS char* a = smem + sl * STB + ki * 64 * ROWB + roff[m] + half * RSTEP;
    if constexpr (half == 0) wlo[r] = tr_read(a); else whi[r] = tr_read(a);
  };
  auto mma1 = [&](auto SL, auto J, auto N) {
    constexpr int sl = decltype(SL)::v, j = decltype(J)::v, n = decltype(N)::v;
    constexpr int ki = j / NWT, m = j % NWT, r = j & 3;
    s16x8 xv = __builtin_bit_cast(s16x8, xf[sl][ki * 4 + n]);
    s16x8 wv = cat8(wlo[r], whi[r]);
    if (G1) mfma_accv(acc[m][n], wv, xv);
    else    mfma_accv(acc[m][n], xv, wv);
  };
  auto pin = [&](auto SL) {
    constexpr int sl = decltype(SL)::v;
    pin4(xf[sl][0], xf[sl][1], xf[sl][2], xf[sl][3]);
    if constexpr (NX == 8) pin4(xf[sl][4], xf[sl][5], xf[sl][6], xf[sl][7]);
  };
  auto stage = [&](auto I, auto ISSUE, auto WAITN, int st_lin, unsigned target, auto EXTRA) {
    constexpr int i = decltype(I)::v, nx = (i + 1) % NS, pv = (i + NS - 1) % NS;
    constexpr int waitn = decltype(WAITN)::v;
    constexpr bool do_issue = decltype(ISSUE)::v;
    unsigned pollv;
    const int st = stage_of(st_lin);
    static_for<0, NF>([&](auto J) {
      constexpr int j = decltype(J)::v, ja = j + LA;
      if constexpr (waitn >= 0 && j == NF - LA) { sb_check<(ABL == 2) ? 0 : 4>(pollv, cnt_addr, target); SB(); }
      mma1(IC<i>{}, J, IC<0>{}); SB();
      if constexpr (waitn >= 0 && j == NF - LA - 2) { sb_poll_issue(pollv, cnt_addr); SB(); }
      if constexpr (ja < NF) readh(IC<i>{}, IC<ja>{}, IC<0>{});
      else if constexpr (waitn >= 0) readh(IC<nx>{}, IC<ja - NF>{}, IC<0>{});
      SB();
      mma1(IC<i>{}, J, IC<1>{}); SB();
      if constexpr (ja < NF) readh(IC<i>{}, IC<ja>{}, IC<1>{});
      else if constexpr (waitn >= 0) readh(IC<nx>{}, IC<ja - NF>{}, IC<1>{});
      SB();
      mma1(IC<i>{}, J, IC<2>{}); SB();
      if constexpr (waitn >= 0 && j == 0) {
        wait_vm<waitn * OPS + decltype(EXTRA)::v>();
        pin(IC<nx>{});
        sb_arrive(cnt_addr, lane);
      }
      if constexpr (do_issue) {
        if constexpr (j >= 1 && j <= 4) issue1(st, IC<pv>{}, IC<4 + (j - 1) * KW>{});
        if constexpr (j >= NF - LA) issue1(st, IC<pv>{}, IC<(j == 5) ? 0 : j - 4>{});
      }
      SB();
      mma1(IC<i>{}, J, IC<3>{}); SB();
      if constexpr (do_issue) {
        if constexpr (KW == 2 && j >= 1 && j <= 4) issue1(st, IC<pv>{}, IC<4 + (j - 1) * KW + 1>{});
        if constexpr (j == NF - LA) issue1(st, IC<pv>{}, IC<1>{});
      }
      SB();
    });
  };

  static_for<0, NS - 1>([&](auto I) {
    const int st = stage_of(decltype(I)::v);
    static_for<0, NX>([&](auto OP) { issue1(st, I, IC<4 + decltype(OP)::v>{}); });
    static_for<0, 4>([&](auto OP) { issue1(st, I, OP); });
  });
  wait_vm<(NS - 2) * OPS>();
  pin(IC<0>{});
  sb_full(cnt_addr, lane, 4);
  STAMPIT(wsdbg, G1 ? 2 : 3, 1);
  SB();
  static_for<0, LA>([&](auto J) { readh(IC<0>{}, J, IC<0>{}); readh(IC<0>{}, J, IC<1>{}); });
  SB();

  for (int tb = 0; tb < NST - NS; tb += NS) {
    static_for<0, NS>([&](auto I) {
      stage(I, IC<1>{}, IC<NS - 3>{}, tb + decltype(I)::v + NS - 1, (unsigned)(tb + decltype(I)::v + 2) * 4, IC<0>{});
    });
  }
  static_for<0, NS>([&](auto I) {
    constexpr int i = decltype(I)::v;
    constexpr int outst = (i == NS - 1) ? -1 : ((i == 0) ? (NS - 3) : (NS - 2 - i));
    stage(I, IC<(i == 0)>{}, IC<outst>{}, NST - 1, (unsigned)(NST - NS + i + 2) * 4, IC<(i == 0) ? 0 : NLATE>{});
    if constexpr (i == 0) { late_loads(); SB(); }
  });
  asm volatile("s_nop 15" ::: "memory");
  SB();
}

template <int NWT>
DEVINL void kpair_reduce(LDS_AS char* smem, f32x4 (&acc)[NWT][4], f32x4 (&fin)[NWT / 2][4], int w, int lane,
                         unsigned cnt_addr, unsigned arrivals  ) {
  constexpr int H = NWT / 2;
  sb_full(cnt_addr, lane, arrivals + 4);
  LDS_AS char* mine = smem + (w * H * 4 * 64 + lane) * 16;
  LDS_AS char* other = smem + ((w ^ 2) * H * 4 * 64 + lane) * 16;
  if (w < 2) {
#pragma unroll
    for (int m = 0; m < H; ++m)
#pragma unroll
      for (int n = 0; n < 4; ++n) { *(LDS_AS f32x4*)(mine + (m * 4 + n) * 1024) = acc[H + m][n]; fin[m][n] = acc[m][n]; }
  } else {
#pragma unroll
    for (int m = 0; m < H; ++m)
#pragma unroll
      for (int n = 0; n < 4; ++n) { *(LDS_AS f32x4*)(mine + (m * 4 + n) * 1024) = acc[m][n]; fin[m][n] = acc[H + m][n]; }
  }
  sb_full(cnt_addr, lane, arrivals + 8);
#pragma unroll
  for (int m = 0; m < H; ++m)
#pragma unroll
    for (int n = 0; n < 4; ++n) fin[m][n] += *(const LDS_AS f32x4*)(other + (m * 4 + n) * 1024);
}

constexpr int G1_NS = G1NS, G2_NS = G2NS;
constexpr int G1_CV = G1_NS * 16384, G1_CNT = G1_CV + CV_LDS, G2_YA = G2_NS * 16384, G2_CNT = G2_YA + 16384;
constexpr int G1_LDS = G1_CNT + 16, G2_LDS = (G2_CNT + 16 > 98304) ? G2_CNT + 16 : 98304  ;

__global__ __launch_bounds__(512, 2) void k_gemm1(const u16* __restrict__ w1, const u16* __restrict__ b1,
                                                  const u16* __restrict__ w2, const u16* __restrict__ ssmk,
                                                  const u16* __restrict__ ssmD, char* __restrict__ ws) {
  extern __shared__ __attribute__((aligned(16))) char dsm[];
  const int b = blockIdx.x, xcd = b & 7, j = b >> 3;
  const int tid = threadIdx.x, lane = tid & 63;
  const int w = __builtin_amdgcn_readfirstlane(tid >> 6);
  const unsigned cnt_addr = (unsigned)(size_t)(LDS_AS char*)dsm + G1_CNT;
  if (tid == 0) *(LDS_AS unsigned*)((LDS_AS char*)dsm + G1_CNT) = 0u;
  __syncthreads();
  if (w >= 4) {
    conv_wave((LDS_AS char*)dsm + G1_CV + (w - 4) * CV_WAVE, (xcd * 32 + j) * 4 + (w - 4), lane, ssmk, ssmD, ws, false);
    return;
  }
  if (j >= 28) {
    const int wv = (b - 224) * 4 + w;
    touch_lines<false>((const char*)w2 + (size_t)wv * 8192, lane * 128, 128 * 8192, PFW2N);
    return;
  }
  const int dt = (xcd & 1) * 4 + (j & 3), ft = (xcd >> 1) * 7 + (j >> 2);
  const int rot = (WIN1 == 8 ? 2 : 1) * (j & 3) + (j >> 2);
  const int g = lane >> 4, wk = w >> 1;
  f32x4 acc[8][4];
#pragma unroll
  for (int m = 0; m < 8; ++m)
#pragma unroll
    for (int n = 0; n < 4; ++n) acc[m][n] = (f32x4){0, 0, 0, 0};
  const char* Xg = ws + WS_HA + (size_t)dt * 4 * 64 * 2048;
  const unsigned xoff = (unsigned)((tid >> 6) & 1) * (2 * 64 * 2048) + (unsigned)(tid >> 7) * 2048 + lane * 16;
  u32x2 bb[4];
  auto late = [&]() {
#pragma unroll
    for (int i = 0; i < 4; ++i) gload8(bb[i], (unsigned)(ft * 128 + wk * 64 + 16 * i + 4 * g) * 2, (const char*)b1);
  };
  STAMPIT(ws, 2, 0);
  gemm_main<true, G1_NS, 32, WIN1, 4>(late, (LDS_AS char*)dsm, (const char*)w1 + ft * 256, F_ * 2, Xg, xoff, xoff + 64 * 2048, acc, w,
                                      lane, rot, cnt_addr, ws);
  STAMPIT(ws, 2, 2);
  asm volatile("s_waitcnt vmcnt(0)" : "+v"(bb[0]), "+v"(bb[1]), "+v"(bb[2]), "+v"(bb[3])::"memory");
  f32x4 fin[4][4];
  kpair_reduce<8>((LDS_AS char*)dsm, acc, fin, w, lane, cnt_addr, 32 * 4);
#pragma unroll
  for (int i = 0; i < 2; ++i) {
    float bias[8] = {bflo(bb[2 * i][0]), bfhi(bb[2 * i][0]), bflo(bb[2 * i][1]), bfhi(bb[2 * i][1]),
                     bflo(bb[2 * i + 1][0]), bfhi(bb[2 * i + 1][0]), bflo(bb[2 * i + 1][1]), bfhi(bb[2 * i + 1][1])};
#pragma unroll
    for (int n = 0; n < 4; ++n) {
      float r[8];
#pragma unroll
      for (int e = 0; e < 8; ++e) {
        float t = fin[2 * i + (e >> 2)][n][e & 3] + bias[e];
        float z = t * (-2.3022081985378543f + -0.10294324068f * t * t);
        float ex = __builtin_amdgcn_exp2f(z);
        r[e] = t * __builtin_amdgcn_rcpf(1.0f + ex);
      }
      u32x4 o = {pack_bf16(r[0], r[1]), pack_bf16(r[2], r[3]), pack_bf16(r[4], r[5]), pack_bf16(r[6], r[7])};
      const int dblk = dt * 4 + (w & 1) * 2 + (n >> 1);
      ((u32x4*)(ws + WS_UA))[((size_t)(dblk * 112 + ft * 4 + wk * 2 + i) * 2 + (n & 1)) * 64 + lane] = o;
    }
  }
  STAMPIT(ws, 2, 3);
}

__global__ __launch_bounds__(256, 2) void k_gemm2(const u16* __restrict__ w2, const u16* __restrict__ b2, const u16* __restrict__ x,
                                                  char* __restrict__ ws, u16* __restrict__ out) {
  extern __shared__ __attribute__((aligned(16))) char dsm[];
  const int b = blockIdx.x, xcd = b & 7, j = b >> 3;
  const int dt = (xcd & 1) * 4 + (j & 3), stl = (xcd >> 1) * 8 + (j >> 2);
  const int rot = ROT2M * (j & 3) + (j >> 2);
  const int tid = threadIdx.x, lane = tid & 63;
  const int w = __builtin_amdgcn_readfirstlane(tid >> 6);
  STAMPIT(ws, 3, 0);
  const unsigned cnt_addr = (unsigned)(size_t)(LDS_AS char*)dsm + G2_CNT;
  if (tid == 0) *(LDS_AS u32x2*)((LDS_AS char*)dsm + G2_CNT) = (u32x2){0u, 0u};
  __syncthreads();
  const int g = lane >> 4, n = lane & 15, q = (lane >> 2) & 3, p = lane & 3, wk = w >> 1;
  const int d0 = dt * 128 + (w & 1) * 64 + 8 * g;
  const int sbase = stl * 64 + wk * 32;
  u32x4 xv[2][2]; u32x2 stv[2]; unsigned bv[2];
  auto late = [&]() {
    {
      const char* ya = ws + WS_YA + (size_t)(dt * 128 + w * 32) * (S_ * 2) + stl * 128;
      const unsigned ldsy = (unsigned)(size_t)(LDS_AS char*)dsm + G2_YA + w * 4096;
      const int r8 = lane >> 3, cp = lane & 7;
      const unsigned off = (unsigned)r8 * (S_ * 2) + 32 * ((cp >> 1) ^ ((r8 >> 1) & 3)) + 16 * (cp & 1);
#pragma unroll
      for (int i = 0; i < 4; ++i) glds16<0, false>(off, ya + (size_t)i * 8 * (S_ * 2), ldsy + i * 1024);
    }
#pragma unroll
    for (int nt = 0; nt < 2; ++nt) {
      const int s = sbase + 16 * nt + n;
      gload16<0, true>(xv[nt][0], (unsigned)(s * D_ + d0) * 2, (const char*)x);
      gload16<64, true>(xv[nt][1], (unsigned)(s * D_ + d0) * 2, (const char*)x);
      gload8(stv[nt], (unsigned)s * 8, ws + WS_STAT);
      gload2(bv[nt], (unsigned)s * 2, (const char*)b2);
    }
  };
  f32x4 acc[4][4];
#pragma unroll
  for (int m = 0; m < 4; ++m)
#pragma unroll
    for (int nn = 0; nn < 4; ++nn) acc[m][nn] = (f32x4){0, 0, 0, 0};
  const char* Xg = ws + WS_UA + (size_t)dt * 4 * 112 * 2048;
  const unsigned xoff = (unsigned)((tid >> 6) & 1) * (2 * 112 * 2048) + (unsigned)(tid >> 7) * 2048 + lane * 16;
  gemm_main<false, G2_NS, 28, WIN2, 12>(late, (LDS_AS char*)dsm, (const char*)w2 + stl * 128, S_ * 2, Xg, xoff, xoff + 112 * 2048,
                                       acc, w, lane, rot, cnt_addr, ws);
  STAMPIT(ws, 3, 2);
  asm volatile("s_waitcnt vmcnt(0)" : "+v"(xv[0][0]), "+v"(xv[0][1]), "+v"(xv[1][0]), "+v"(xv[1][1]), "+v"(stv[0]), "+v"(stv[1]),
               "+v"(bv[0]), "+v"(bv[1])::"memory");
  f32x4 fin[2][4];
  kpair_reduce<4>((LDS_AS char*)dsm, acc, fin, w, lane, cnt_addr, 28 * 4);
  STAMPIT(ws, 3, 3);
#pragma unroll
  for (int nt = 0; nt < 2; ++nt) {
    const int s = sbase + 16 * nt + n;
#pragma unroll
    for (int h = 0; h < 2; ++h) {
      s16x4 yv[2];
#pragma unroll
      for (int r = 0; r < 2; ++r) {
        const int row = 64 * (w & 1) + 32 * h + 8 * g + 4 * r + q;
        yv[r] = tr_read((LDS_AS char*)dsm + G2_YA + row * 128 + 32 * ((2 * wk + nt) ^ ((row >> 1) & 3)) + 8 * p);
      }
      float r[8];
#pragma unroll
      for (int e = 0; e < 8; ++e) {
        float xe = (e & 1) ? bfhi(xv[nt][h][e >> 1]) : bflo(xv[nt][h][e >> 1]);
        float ye = bf2f((unsigned)(u16)yv[e >> 2][e & 3]);
        float xn = rbf((xe - __uint_as_float(stv[nt][0])) * __uint_as_float(stv[nt][1]));
        r[e] = xe + xn * ye + (fin[nt][2 * h + (e >> 2)][e & 3] + bf2f(bv[nt]));
      }
      u32x4 o = {pack_bf16(r[0], r[1]), pack_bf16(r[2], r[3]), pack_bf16(r[4], r[5]), pack_bf16(r[6], r[7])};
      __builtin_nontemporal_store(o, (u32x4*)(out + (size_t)s * D_ + d0 + 32 * h));
    }
  }
  STAMPIT(ws, 3, 4);
}

extern "C" void kernel_launch(void* const* d_in, const int* in_sizes, int n_in, void* d_out, int out_size, void* d_ws,
                              size_t ws_size, hipStream_t stream) {
  const u16* x = (const u16*)d_in[0];
  const u16* k = (const u16*)d_in[1];
  const u16* v = (const u16*)d_in[2];
  const u16* shk = (const u16*)d_in[3];
  const u16* ssmk = (const u16*)d_in[4];
  const u16* ssmD = (const u16*)d_in[5];
  const u16* w1 = (const u16*)d_in[7];
  const u16* b1 = (const u16*)d_in[8];
  const u16* w2 = (const u16*)d_in[9];
  const u16* b2 = (const u16*)d_in[10];
  char* ws = (char*)d_ws;
  (void)hipFuncSetAttribute((const void*)k_gemm1, hipFuncAttributeMaxDynamicSharedMemorySize, G1_LDS);
  (void)hipFuncSetAttribute((const void*)k_gemm2, hipFuncAttributeMaxDynamicSharedMemorySize, G2_LDS);
  k_prologue<<<256, 576, 0, stream>>>(x, k, v, shk, w1, ws);
  k_gemm1<<<256, 512, G1_LDS, stream>>>(w1, b1, w2, ssmk, ssmD, ws);
  k_gemm2<<<256, 256, G2_LDS, stream>>>(w2, b2, x, ws, (u16*)d_out);
}
```

```cpp
#include <hip/hip_runtime.h>
#include <hip/hip_bf16.h>
#include <utility>
#define ABL 0
#define PF2 1
#define WIN1 4
#define WIN2 4
#define G1NS 4
#define G2NS 4
#define PF1 0
#define PF1ROWS 512
#define G2HWBAR true
#define LEAD1 false
#define LEAD2 false
#define ROT2M 1
#define PFA 0
#define PFADELAY 60
#define PFW1N 0
#define PFW2N 14
#define GPRIO 0
#define POLLSTAGE 24

typedef short    s16x8 __attribute__((ext_vector_type(8)));
typedef short    s16x4 __attribute__((ext_vector_type(4)));
typedef float    f32x4 __attribute__((ext_vector_type(4)));
typedef float    f32x2 __attribute__((ext_vector_type(2)));
typedef unsigned u32x4 __attribute__((ext_vector_type(4)));
typedef unsigned u32x2 __attribute__((ext_vector_type(2)));
typedef unsigned short u16;
#define LDS_AS __attribute__((address_space(3)))
#define DEVINL __device__ __forceinline__
typedef __attribute__((address_space(1))) unsigned gu32;
typedef __attribute__((address_space(1))) unsigned long long gu64;

constexpr int S_ = 2048, D_ = 1024, F_ = 3584;

constexpr size_t WS_HA   = 0;
constexpr size_t WS_KVT  = WS_HA  + (size_t)S_ * D_ * 2;
constexpr size_t WS_YA   = WS_KVT + (size_t)S_ * D_ * 2;
constexpr size_t WS_UA   = WS_YA  + (size_t)S_ * D_ * 2;
constexpr size_t WS_STAT = WS_UA  + (size_t)D_ * F_ * 2;
constexpr size_t WS_FLAG = WS_STAT + (size_t)S_ * 8;

DEVINL float bf2f(unsigned v16) { return __uint_as_float(v16 << 16); }
DEVINL float bflo(unsigned v) { return __uint_as_float(v << 16); }
DEVINL float bfhi(unsigned v) { return __uint_as_float(v & 0xffff0000u); }
DEVINL unsigned pack_bf16(float lo, float hi) {
  typedef __bf16 bf2 __attribute__((ext_vector_type(2)));
  f32x2 v = {lo, hi};
  bf2 r = __builtin_convertvector(v, bf2);
  return __builtin_bit_cast(unsigned, r);
}
DEVINL float rbf(float x) { return bflo(pack_bf16(x, x)); }

template <int N> DEVINL void wait_vm() { asm volatile("s_waitcnt vmcnt(%0)" ::"n"(N) : "memory"); }
template <int I> struct IC { static constexpr int v = I; };
template <int B, int E, typename Fn> DEVINL void static_for(Fn&& f) {
  if constexpr (B < E) { f(IC<B>{}); static_for<B + 1, E>(f); }
}
DEVINL float wave_sum(float v) {
#pragma unroll
  for (int o = 32; o > 0; o >>= 1) v += __shfl_xor(v, o, 64);
  return v;
}
DEVINL s16x4 tr_read(const LDS_AS char* p) {
  return __builtin_amdgcn_ds_read_tr16_b64_v4i16((LDS_AS s16x4*)p);
}
template <int OFF, bool NT> DEVINL void gload16(u32x4& dst, unsigned voff, const char* sbase) {
  if constexpr (NT) asm volatile("global_load_dwordx4 %0, %1, %2 offset:%3 nt" : "=v"(dst) : "v"(voff), "s"(sbase), "n"(OFF));
  else asm volatile("global_load_dwordx4 %0, %1, %2 offset:%3" : "=v"(dst) : "v"(voff), "s"(sbase), "n"(OFF));
}
template <int OFF, bool NT> DEVINL void glds16(unsigned voff, const char* sbase, unsigned ldsaddr) {
  if constexpr (NT) asm volatile("s_mov_b32 m0, %2\n\ts_nop 0\n\tglobal_load_lds_dwordx4 %0, %1 offset:%3 nt" ::"v"(voff), "s"(sbase), "s"(ldsaddr), "n"(OFF) : "memory");
  else asm volatile("s_mov_b32 m0, %2\n\ts_nop 0\n\tglobal_load_lds_dwordx4 %0, %1 offset:%3" ::"v"(voff), "s"(sbase), "s"(ldsaddr), "n"(OFF) : "memory");
}
template <int OFF, bool NT> DEVINL void glds16_more(unsigned voff, const char* sbase) {
  if constexpr (NT) asm volatile("global_load_lds_dwordx4 %0, %1 offset:%2 nt" ::"v"(voff), "s"(sbase), "n"(OFF) : "memory");
  else asm volatile("global_load_lds_dwordx4 %0, %1 offset:%2" ::"v"(voff), "s"(sbase), "n"(OFF) : "memory");
}
DEVINL void mfma_acc(f32x4& c, s16x8 a, s16x8 b) {
  asm volatile("v_mfma_f32_16x16x32_bf16 %0, %1, %2, %0" : "+a"(c) : "v"(a), "v"(b));
}
DEVINL void mfma_accv(f32x4& c, s16x8 a, s16x8 b) {
  asm volatile("v_mfma_f32_16x16x32_bf16 %0, %1, %2, %0" : "+v"(c) : "v"(a), "v"(b));
}
DEVINL void pfload(unsigned& dummy, unsigned voff, const char* sbase) {
  asm volatile("global_load_dword %0, %1, %2" : "+v"(dummy) : "v"(voff), "s"(sbase));
}
DEVINL void sb_arrive(unsigned cnt_addr, int lane) {
  unsigned one = 1u;
  asm volatile("s_mov_b64 exec, 1\n\tds_add_u32 %0, %1\n\ts_mov_b64 exec, -1" ::"v"(cnt_addr), "v"(one) : "memory");
}
DEVINL void sb_poll_issue(unsigned& pv, unsigned cnt_addr) { asm volatile("ds_read_b32 %0, %1" : "=v"(pv) : "v"(cnt_addr) : "memory"); }
template <int YOUNGER> DEVINL void sb_check(unsigned& pv, unsigned cnt_addr, unsigned target) {
  asm volatile("s_waitcnt lgkmcnt(%1)" : "+v"(pv) : "n"(YOUNGER) : "memory");
  unsigned v = __builtin_amdgcn_readfirstlane(pv);
  while (v < target) {
    __builtin_amdgcn_s_sleep(1);
    unsigned t;
    asm volatile("ds_read_b32 %0, %1\n\ts_waitcnt lgkmcnt(0)" : "=v"(t) : "v"(cnt_addr) : "memory");
    v = __builtin_amdgcn_readfirstlane(t);
  }
}
DEVINL void sb_full(unsigned cnt_addr, int lane, unsigned target) {
  asm volatile("s_waitcnt lgkmcnt(0)" ::: "memory");
  sb_arrive(cnt_addr, lane);
  unsigned pv;
  sb_poll_issue(pv, cnt_addr);
  sb_check<0>(pv, cnt_addr, target);
}
DEVINL void gload8(u32x2& dst, unsigned voff, const char* sbase) {
  asm volatile("global_load_dwordx2 %0, %1, %2" : "=v"(dst) : "v"(voff), "s"(sbase));
}
DEVINL void gload2(unsigned& dst, unsigned voff, const char* sbase) {
  asm volatile("global_load_ushort %0, %1, %2" : "=v"(dst) : "v"(voff), "s"(sbase));
}
DEVINL void pin2(u32x4& a, u32x4& b) { asm volatile("" : "+v"(a), "+v"(b)); }
template <typename T> DEVINL T ldnt(const void* p) { return __builtin_nontemporal_load((const T*)p); }
DEVINL float dppf(float v, int ctrl, int rmask, bool bc) { return v; }
template <int CTRL, int RM, bool BC> DEVINL float dpp0(float v) {
  return __builtin_bit_cast(float, __builtin_amdgcn_update_dpp(0, __builtin_bit_cast(int, v), CTRL, RM, 0xf, BC));
}
DEVINL float wave_total(float v) {
  v += dpp0<0x111, 0xf, true>(v); v += dpp0<0x112, 0xf, true>(v); v += dpp0<0x114, 0xf, true>(v); v += dpp0<0x118, 0xf, true>(v);
  v += dpp0<0x142, 0xa, false>(v); v += dpp0<0x143, 0xc, false>(v);
  return __builtin_bit_cast(float, __builtin_amdgcn_readlane(__builtin_bit_cast(int, v), 63));
}
template <bool NT> DEVINL void touch1(unsigned& dummy, unsigned voff, const char* base) {
  if constexpr (NT) asm volatile("global_load_dword %0, %1, %2 nt" : "+v"(dummy) : "v"(voff), "s"(base));
  else asm volatile("global_load_dword %0, %1, %2" : "+v"(dummy) : "v"(voff), "s"(base));
}
template <bool NT> DEVINL void touch_lines(const char* base, unsigned voff, unsigned stride, int n) {
  unsigned dummy = 0u;
  for (int i = 0; i < n; ++i) { touch1<NT>(dummy, voff, base); voff += stride; }
  asm volatile("s_waitcnt vmcnt(0)" : "+v"(dummy)::"memory");
}
DEVINL void pin4(u32x4& a, u32x4& b, u32x4& c, u32x4& d) { asm volatile("" : "+v"(a), "+v"(b), "+v"(c), "+v"(d)); }
DEVINL s16x8 cat8(s16x4 a, s16x4 b) { return __builtin_shufflevector(a, b, 0, 1, 2, 3, 4, 5, 6, 7); }

#define STAMPIT(ws, k, i)
__global__ __launch_bounds__(576) void k_prologue(const u16* __restrict__ x, const u16* __restrict__ kin,
                                                  const u16* __restrict__ vin, const u16* __restrict__ shk,
                                                  const u16* __restrict__ w1, char* __restrict__ ws) {
  __shared__ __attribute__((aligned(16))) u16 sh[8 * 1024];
  const int b = blockIdx.x, tid = threadIdx.x, lane = tid & 63;
  const int w = __builtin_amdgcn_readfirstlane(tid >> 6);
  const int s0 = b * 8;
  STAMPIT(ws, 0, 0);
  if (w == 8) {
    return;
  }
  const u16* xr = x + (size_t)(s0 + w) * D_;
  u32x4 xa = ldnt<u32x4>(xr + lane * 8);
  u32x4 xb = ldnt<u32x4>(xr + 512 + lane * 8);
  const int ks0 = (b >> 3) * 64 + w * 8, kd = (b & 7) * 128 + 2 * lane;
  unsigned kk[11], vv[8];
#pragma unroll
  for (int i = 0; i < 11; ++i) {
    int s = ks0 - 3 + i;
    kk[i] = ldnt<unsigned>(kin + (size_t)(s < 0 ? 0 : s) * D_ + kd);
    if (i < 3 && s < 0) kk[i] = 0u;
  }
#pragma unroll
  for (int i = 0; i < 8; ++i) vv[i] = ldnt<unsigned>(vin + (size_t)(ks0 + i) * D_ + kd);
  u32x4 sk = *(const u32x4*)(shk + (size_t)kd * 4);

  f32x2 f[8];
#pragma unroll
  for (int i = 0; i < 4; ++i) { f[i] = (f32x2){bflo(xa[i]), bfhi(xa[i])}; f[4 + i] = (f32x2){bflo(xb[i]), bfhi(xb[i])}; }
  unsigned hp[8];
#pragma unroll
  for (int pass = 0; pass < 2; ++pass) {
    f32x2 s = f[0];
#pragma unroll
    for (int i = 1; i < 8; ++i) s += f[i];
    const float mean = wave_total(s[0] + s[1]) * (1.0f / D_);
    f32x2 q = {0.f, 0.f};
#pragma unroll
    for (int i = 0; i < 8; ++i) { f32x2 dl = f[i] - mean; q += dl * dl; }
    const float var = wave_total(q[0] + q[1]) * (1.0f / D_);
    const float mb = rbf(mean);
    const float rinv = 1.0f / rbf(sqrtf(rbf(rbf(var) + 1e-5f)));
#pragma unroll
    for (int i = 0; i < 8; ++i) {
      f32x2 t = (f[i] - mb) * rinv;
      hp[i] = pack_bf16(t[0], t[1]);
      if (pass == 0) f[i] = (f32x2){bflo(hp[i]), bfhi(hp[i])};
    }
    if (pass == 0 && lane == 0) { f32x2 st = {mb, rinv}; *(f32x2*)(ws + WS_STAT + (size_t)(s0 + w) * 8) = st; }
  }
  *(u32x4*)(sh + w * 1024 + lane * 8) = (u32x4){hp[0], hp[1], hp[2], hp[3]};
  *(u32x4*)(sh + w * 1024 + 512 + lane * 8) = (u32x4){hp[4], hp[5], hp[6], hp[7]};
  {
    const f32x2 tp[4] = {{bflo(sk[0]), bflo(sk[2])}, {bfhi(sk[0]), bfhi(sk[2])}, {bflo(sk[1]), bflo(sk[3])}, {bfhi(sk[1]), bfhi(sk[3])}};
    f32x2 kf2[11], r[8];
#pragma unroll
    for (int i = 0; i < 11; ++i) kf2[i] = (f32x2){bflo(kk[i]), bfhi(kk[i])};
#pragma unroll
    for (int i = 0; i < 8; ++i) {
      f32x2 a = kf2[i] * tp[3];
      a += kf2[i + 1] * tp[2];
      a += kf2[i + 2] * tp[1];
      a += kf2[i + 3] * tp[0];
      r[i] = a * (f32x2){bflo(vv[i]), bfhi(vv[i])};
    }
    u32x4 o0, o1;
#pragma unroll
    for (int i = 0; i < 4; ++i) { o0[i] = pack_bf16(r[2 * i][0], r[2 * i + 1][0]); o1[i] = pack_bf16(r[2 * i][1], r[2 * i + 1][1]); }
    u32x4* dst = (u32x4*)(ws + WS_KVT + ((size_t)kd * S_ + ks0) * 2);
    dst[0] = o0; dst[S_ / 8] = o1;
  }
  STAMPIT(ws, 0, 1);
  __syncthreads();
  {
    unsigned c[8];
#pragma unroll
    for (int i = 0; i < 8; ++i) c[i] = *(const unsigned*)(sh + i * 1024 + 2 * tid);
    u32x4 o0, o1;
#pragma unroll
    for (int i = 0; i < 4; ++i) {
      o0[i] = __builtin_amdgcn_perm(c[2 * i + 1], c[2 * i], 0x05040100u);
      o1[i] = __builtin_amdgcn_perm(c[2 * i + 1], c[2 * i], 0x07060302u);
    }
    const int d = 2 * tid, dblk = d >> 5, n = (d >> 2) & 1, cc = 4 * ((d >> 3) & 3) + (d & 3);
    const int kstep = b >> 2, g = b & 3;
    u32x4* dst = (u32x4*)(ws + WS_HA) + ((size_t)(dblk * 64 + kstep) * 2 + n) * 64 + cc + 16 * g;
    dst[0] = o0; dst[1] = o1;
  }
  STAMPIT(ws, 0, 2);
}

constexpr int CV_XPOFF = 4160;
constexpr int CV_XP = 1152;
constexpr int CV_WAVE = CV_XPOFF + 4 * CV_XP;
constexpr int CV_LDS = 4 * CV_WAVE;

DEVINL void conv_wave(LDS_AS char* kf, int d, int lane, const u16* __restrict__ ssmk, const u16* __restrict__ ssmD,
                      char* __restrict__ ws, bool bar) {
  LDS_AS char* xp = kf + CV_XPOFF;
  u32x4 kfv[4], kvv[4];
#pragma unroll
  for (int i = 0; i < 4; ++i) kfv[i] = ldnt<u32x4>(ssmk + (size_t)d * S_ + (i * 64 + lane) * 8);
#pragma unroll
  for (int i = 0; i < 4; ++i) kvv[i] = *((const u32x4*)(ws + WS_KVT + (size_t)d * (S_ * 2)) + i * 64 + lane);
  const float Dd = bf2f(ssmD[d]);
  if (lane < 8) *(LDS_AS unsigned*)(kf + lane * 4) = 0u;
  {
    u32x2 z = {0u, 0u};
    *(LDS_AS u32x2*)(xp + (lane >> 4) * CV_XP + (lane & 15) * 8) = z;
  }
  if (bar) { __builtin_amdgcn_s_barrier(); __builtin_amdgcn_s_barrier(); __builtin_amdgcn_s_barrier(); }
#pragma unroll
  for (int i = 0; i < 4; ++i) *(LDS_AS u32x4*)(kf + 32 + (i * 64 + lane) * 16) = kfv[i];
#pragma unroll
  for (int i = 0; i < 4; ++i) {
    const int sb = i * 64 + lane;
    unsigned p0 = __builtin_amdgcn_perm(kvv[i][2], kvv[i][0], 0x05040100u);
    unsigned p1 = __builtin_amdgcn_perm(kvv[i][2], kvv[i][0], 0x07060302u);
    unsigned p2 = __builtin_amdgcn_perm(kvv[i][3], kvv[i][1], 0x05040100u);
    unsigned p3 = __builtin_amdgcn_perm(kvv[i][3], kvv[i][1], 0x07060302u);
    *(LDS_AS unsigned*)(xp + 0 * CV_XP + 128 + sb * 4) = p0;
    *(LDS_AS unsigned*)(xp + 1 * CV_XP + 128 + sb * 4) = p1;
    *(LDS_AS unsigned*)(xp + 2 * CV_XP + 128 + sb * 4) = p2;
    *(LDS_AS unsigned*)(xp + 3 * CV_XP + 128 + sb * 4) = p3;
  }
  __builtin_amdgcn_s_waitcnt(0xC07F);
  __builtin_amdgcn_wave_barrier();
  if (bar) __builtin_amdgcn_s_barrier();
  const int g = lane >> 4, q = (lane >> 2) & 3, p = lane & 3, n = lane & 15;
  s16x8 af[16];
  {
    const LDS_AS char* a0 = kf + 2 * (4 * (8 * g + 7 - q) + 4 + 4 * p);
#pragma unroll
    for (int st = 0; st < 16; ++st) {
      s16x4 lo = tr_read(a0 + st * 256);
      s16x4 hi = tr_read(a0 + st * 256 - 32);
      af[st] = cat8(lo, hi);
    }
  }
  const LDS_AS char* b0 = xp + 2 * (64 + 4 * n - 8 * g - 4);
  const int src = ((lane >= 16) ? (lane - 16) : (lane == 0 ? 63 : lane + 47)) * 4;
  float prev0 = 0.f, prev1 = 0.f, prev2 = 0.f;
  u32x2* yrow = (u32x2*)(ws + WS_YA + (size_t)d * (S_ * 2)) + 4 * n + g;
#pragma unroll
  for (int np = 0; np < 4; ++np) {
    asm volatile("" ::: "memory");
    __builtin_amdgcn_sched_barrier(0);
    f32x4 acc[4][2];
#pragma unroll
    for (int ph = 0; ph < 4; ++ph) { acc[ph][0] = (f32x4){0.f, 0.f, 0.f, 0.f}; acc[ph][1] = (f32x4){0.f, 0.f, 0.f, 0.f}; }
#pragma unroll
    for (int beta = -1; beta <= 4 * np + 2; ++beta) {
#pragma unroll
      for (int ph = 0; ph < 4; ++ph) {
        const LDS_AS char* bp = b0 + ph * CV_XP + beta * 64;
        s16x4 lo = *(const LDS_AS s16x4*)(bp);
        s16x4 hi = *(const LDS_AS s16x4*)(bp + 8);
        s16x8 bf = cat8(lo, hi);
#pragma unroll
        for (int h = 0; h < 2; ++h) {
          const int step = 4 * np + 2 * h - beta;
          if (step >= 0 && step <= 15)
            acc[ph][h] = __builtin_amdgcn_mfma_f32_16x16x32_bf16(af[step], bf, acc[ph][h], 0, 0, 0);
        }
      }
      if (bar && (beta & 1) == 0) __builtin_amdgcn_s_barrier();
    }
#pragma unroll
    for (int h = 0; h < 2; ++h) {
      const int nu = 2 * np + h;
      float c0 = acc[1][h][3] + acc[2][h][2] + acc[3][h][1];
      float c1 = acc[2][h][3] + acc[3][h][2];
      float c2 = acc[3][h][3];
      float p0 = __int_as_float(__builtin_amdgcn_ds_bpermute(src, __float_as_int(c0)));
      float p1 = __int_as_float(__builtin_amdgcn_ds_bpermute(src, __float_as_int(c1)));
      float p2 = __int_as_float(__builtin_amdgcn_ds_bpermute(src, __float_as_int(c2)));
      float a0 = (lane == 0) ? prev0 : p0, a1 = (lane == 0) ? prev1 : p1, a2 = (lane == 0) ? prev2 : p2;
      prev0 = p0; prev1 = p1; prev2 = p2;
      float y[4];
      y[0] = acc[0][h][0] + a0;
      y[1] = acc[0][h][1] + acc[1][h][0] + a1;
      y[2] = acc[0][h][2] + acc[1][h][1] + acc[2][h][0] + a2;
      y[3] = acc[0][h][3] + acc[1][h][2] + acc[2][h][1] + acc[3][h][0];
#pragma unroll
      for (int r = 0; r < 4; ++r) {
        float kvv_ = bf2f(*(const LDS_AS u16*)(xp + r * CV_XP + 128 + 2 * (64 * nu + 4 * n + g)));
        y[r] += kvv_ * Dd;
      }
      u32x2 o = {pack_bf16(y[0], y[1]), pack_bf16(y[2], y[3])};
      yrow[64 * nu] = o;
    }
  }
}

#define SB() __builtin_amdgcn_sched_barrier(0)
template <bool G1, int NS, int NST, int WIN, int NLATE, typename Hook>
DEVINL void gemm_main(Hook&& late_loads  ,
                      LDS_AS char* smem, const char* __restrict__ Wg  , const unsigned wrow  ,
                      const char* __restrict__ Xg  , unsigned xoff0, unsigned xoff1  ,
                      f32x4 (&acc)[G1 ? 8 : 4][4], int w, int lane, int rot  ,
                      unsigned cnt_addr, char* wsdbg) {
  constexpr int NWT = G1 ? 8 : 4;
  constexpr int ROWB = NWT * 32;
  constexpr int STB = 16384;
  constexpr int KROWS = STB / ROWB;
  constexpr int KW = KROWS / 64;
  constexpr int NX = 4 * KW;
  constexpr int OPS = 4 + NX;
  constexpr bool HWBAR = true;
  constexpr int NF = 8;
  constexpr int LA = 3;
  static_assert(NST % NS == 0 && NS * NX * 4 + NWT * 16 <= 192, "register budget: in-flight X fragments must never be spilled");
  const int g = lane >> 4, q = (lane >> 2) & 3, p = lane & 3, wk = w >> 1;
  unsigned woff[4];
#pragma unroll
  for (int i = 0; i < 4; ++i) {
    int row, cp, key;
    if (G1) { row = 16 * w + 4 * i + (lane >> 4); cp = lane & 15; key = (row & 3) | (((row >> 3) & 1) << 2); }
    else    { row = 32 * w + 8 * i + (lane >> 3); cp = lane & 7;  key = (row >> 1) & 3; }
    woff[i] = (unsigned)row * wrow + 32 * ((cp >> 1) ^ key) + 16 * (cp & 1) - i * 1024;
  }
  const unsigned ldsw = (unsigned)(size_t)smem + w * 4096;
  unsigned roff[NWT];
#pragma unroll
  for (int m = 0; m < NWT; ++m) {
    const int row = G1 ? (8 * g + q) : (4 * g + q);
    const int key = G1 ? (q | ((g & 1) << 2)) : ((row >> 1) & 3);
    roff[m] = (row + 32 * wk) * ROWB + 32 * ((m ^ (wk * (NWT / 2))) ^ key) + 8 * p;
  }
  constexpr int RSTEP = G1 ? 4 * ROWB : 16 * ROWB;
  u32x4 xf[NS][NX];
  s16x4 wlo[4], whi[4];

  auto stage_of = [&](int st_lin) {
    int st = st_lin;
    if constexpr (WIN > 1) {
      const int wb = st_lin & ~(WIN - 1);
      const int msk = (NST - wb >= WIN) ? (WIN - 1) : (NST % WIN - 1);
      st = wb + ((st_lin + rot) & msk);
    }
    return st;
  };
  static_assert(WIN == NS, "");
  const char* wp[NS];
  const char* xp[NS];
#pragma unroll
  for (int i = 0; i < NS; ++i) {
    const int st = stage_of(i);
    wp[i] = Wg + (size_t)st * KROWS * wrow;
    xp[i] = Xg + (size_t)st * (KW * 4096) + 4096;
  }
  auto issue1 = [&](int, auto SL, auto OP) {
    constexpr int sl = decltype(SL)::v, op = decltype(OP)::v;
    if constexpr (op == 0) glds16<0, false>(woff[0], wp[sl], ldsw + sl * STB);
    else if constexpr (op < 4) glds16_more<op * 1024, false>(woff[op], wp[sl]);
    else {
      constexpr int xi = op - 4, ki = xi >> 2, h = (xi >> 1) & 1, n = xi & 1;
      gload16<ki * 4096 + n * 1024 - 4096, false>(xf[sl][xi], h ? xoff1 : xoff0, xp[sl]);
    }
    if constexpr (op == 3) wp[sl] += (size_t)NS * KROWS * wrow;
    if constexpr (op == 4 + NX - 1) xp[sl] += (size_t)NS * (KW * 4096);
  };
  auto readh = [&](auto SL, auto J, auto HALF) {
    constexpr int sl = decltype(SL)::v, j = decltype(J)::v, half = decltype(HALF)::v;
    constexpr int ki = j / NWT, m = j % NWT, r = j & 3;
    const LDS_AS char* a = smem + sl * STB + ki * 64 * ROWB + roff[m] + half * RSTEP;
    if constexpr (half == 0) wlo[r] = tr_read(a); else whi[r] = tr_read(a);
  };
  auto mma1 = [&](auto SL, auto J, auto N) {
    constexpr int sl = decltype(SL)::v, j = decltype(J)::v, n = decltype(N)::v;
    constexpr int ki = j / NWT, m = j % NWT, r = j & 3;
    s16x8 xv = __builtin_bit_cast(s16x8, xf[sl][ki * 4 + n]);
    s16x8 wv = cat8(wlo[r], whi[r]);
    if (G1) mfma_accv(acc[m][n], wv, xv);
    else    mfma_accv(acc[m][n], xv, wv);
  };
  auto pin = [&](auto SL) {
    constexpr int sl = decltype(SL)::v;
    pin4(xf[sl][0], xf[sl][1], xf[sl][2], xf[sl][3]);
    if constexpr (NX == 8) pin4(xf[sl][4], xf[sl][5], xf[sl][6], xf[sl][7]);
  };
  auto stage = [&](auto I, auto ISSUE, auto WAITN, int st_lin, unsigned target, auto EXTRA) {
    constexpr int i = decltype(I)::v, nx = (i + 1) % NS, pv = (i + NS - 1) % NS;
    constexpr int waitn = decltype(WAITN)::v;
    constexpr bool do_issue = decltype(ISSUE)::v;
    unsigned pollv;
    const int st = 0;
    static_for<0, NF>([&](auto J) {
      constexpr int j = decltype(J)::v, ja = j + LA;
      if constexpr (waitn >= 0 && j == NF - LA) {
        if constexpr (HWBAR) {
          wait_vm<waitn * OPS + decltype(EXTRA)::v + (do_issue ? NX : 0)>();
          pin(IC<nx>{});
          __builtin_amdgcn_s_barrier();
        } else {
          sb_check<(ABL == 2) ? 0 : 4>(pollv, cnt_addr, target);
        }
        SB();
      }
      mma1(IC<i>{}, J, IC<0>{}); SB();
      if constexpr (!HWBAR && waitn >= 0 && j == NF - LA - 2) { sb_poll_issue(pollv, cnt_addr); SB(); }
      if constexpr (ja < NF) readh(IC<i>{}, IC<ja>{}, IC<0>{});
      else if constexpr (waitn >= 0) readh(IC<nx>{}, IC<ja - NF>{}, IC<0>{});
      SB();
      mma1(IC<i>{}, J, IC<1>{}); SB();
      if constexpr (ja < NF) readh(IC<i>{}, IC<ja>{}, IC<1>{});
      else if constexpr (waitn >= 0) readh(IC<nx>{}, IC<ja - NF>{}, IC<1>{});
      SB();
      mma1(IC<i>{}, J, IC<2>{}); SB();
      if constexpr (!HWBAR && waitn >= 0 && j == 0) {
        wait_vm<waitn * OPS + decltype(EXTRA)::v>();
        pin(IC<nx>{});
        sb_arrive(cnt_addr, lane);
      }
      if constexpr (do_issue) {
        if constexpr (j >= 1 && j <= 4) issue1(st, IC<pv>{}, IC<4 + (j - 1) * KW>{});
        if constexpr (j >= NF - LA) issue1(st, IC<pv>{}, IC<(j == 5) ? 0 : j - 4>{});
      }
      SB();
      mma1(IC<i>{}, J, IC<3>{}); SB();
      if constexpr (do_issue) {
        if constexpr (KW == 2 && j >= 1 && j <= 4) issue1(st, IC<pv>{}, IC<4 + (j - 1) * KW + 1>{});
        if constexpr (j == NF - LA) issue1(st, IC<pv>{}, IC<1>{});
      }
      SB();
    });
  };

  static_for<0, NS - 1>([&](auto I) {
    const int st = 0;
    static_for<0, NX>([&](auto OP) { issue1(st, I, IC<4 + decltype(OP)::v>{}); });
    static_for<0, 4>([&](auto OP) { issue1(st, I, OP); });
  });
  wait_vm<(NS - 2) * OPS>();
  pin(IC<0>{});
  __builtin_amdgcn_s_barrier();
  STAMPIT(wsdbg, G1 ? 2 : 3, 1);
  SB();
  static_for<0, LA>([&](auto J) { readh(IC<0>{}, J, IC<0>{}); readh(IC<0>{}, J, IC<1>{}); });
  SB();

  for (int tb = 0; tb < NST - NS; tb += NS) {
    static_for<0, NS>([&](auto I) {
      stage(I, IC<1>{}, IC<NS - 3>{}, tb + decltype(I)::v + NS - 1, (unsigned)(tb + decltype(I)::v + 2) * 4, IC<0>{});
    });
  }
  static_for<0, NS>([&](auto I) {
    constexpr int i = decltype(I)::v;
    constexpr int outst = (i == NS - 1) ? -1 : ((i == 0) ? (NS - 3) : (NS - 2 - i));
    stage(I, IC<(i == 0)>{}, IC<outst>{}, NST - 1, (unsigned)(NST - NS + i + 2) * 4, IC<(i == 0) ? 0 : NLATE>{});
    if constexpr (i == 0) { late_loads(); SB(); }
  });
  asm volatile("s_nop 15" ::: "memory");
  SB();
}

template <int NWT>
DEVINL void kpair_reduce(LDS_AS char* smem, f32x4 (&acc)[NWT][4], f32x4 (&fin)[NWT / 2][4], int w, int lane) {
  constexpr int H = NWT / 2;
  __syncthreads();
  LDS_AS char* mine = smem + (w * H * 4 * 64 + lane) * 16;
  LDS_AS char* other = smem + ((w ^ 2) * H * 4 * 64 + lane) * 16;
#pragma unroll
  for (int m = 0; m < H; ++m)
#pragma unroll
    for (int n = 0; n < 4; ++n) *(LDS_AS f32x4*)(mine + (m * 4 + n) * 1024) = acc[H + m][n];
  __syncthreads();
#pragma unroll
  for (int m = 0; m < H; ++m)
#pragma unroll
    for (int n = 0; n < 4; ++n) fin[m][n] = acc[m][n] + *(const LDS_AS f32x4*)(other + (m * 4 + n) * 1024);
}

constexpr int G1_NS = G1NS, G2_NS = G2NS;
constexpr int G1_CNT = G1_NS * 16384, G2_YA = G2_NS * 16384, G2_CNT = G2_YA + 16384;
constexpr int G1_LDS = G1_CNT + 16, G2_LDS = (G2_CNT + 16 > 98304) ? G2_CNT + 16 : 98304  ;

__global__ __launch_bounds__(256, 2) void k_gemm1(const u16* __restrict__ w1, const u16* __restrict__ b1,
                                                  const u16* __restrict__ w2, const u16* __restrict__ ssmk,
                                                  const u16* __restrict__ ssmD, char* __restrict__ ws) {
  extern __shared__ __attribute__((aligned(16))) char dsm[];
  const int b = blockIdx.x & 255, xcd = b & 7, j = b >> 3;
  const int tid = threadIdx.x, lane = tid & 63;
  const int w = __builtin_amdgcn_readfirstlane(tid >> 6);
  if (blockIdx.x >= 256) {
    conv_wave((LDS_AS char*)dsm + w * CV_WAVE, (xcd * 32 + j) * 4 + w, lane, ssmk, ssmD, ws, false);
    return;
  }
  const unsigned cnt_addr = (unsigned)(size_t)(LDS_AS char*)dsm + G1_CNT;
  if (tid == 0) *(LDS_AS unsigned*)((LDS_AS char*)dsm + G1_CNT) = 0u;
  __syncthreads();
  if (j >= 28) {
    const int wv = (b - 224) * 4 + w;
    touch_lines<false>((const char*)w2 + (size_t)wv * 8192, lane * 128, 128 * 8192, PFW2N);
    return;
  }
  const int dt = (xcd & 1) * 4 + (j & 3), ft = (xcd >> 1) * 7 + (j >> 2);
  const int rot = (WIN1 == 8 ? 2 : 1) * (j & 3) + (j >> 2);
  const int g = lane >> 4, wk = w >> 1;
  f32x4 acc[8][4];
#pragma unroll
  for (int m = 0; m < 8; ++m)
#pragma unroll
    for (int n = 0; n < 4; ++n) acc[m][n] = (f32x4){0, 0, 0, 0};
  const char* Xg = ws + WS_HA + (size_t)dt * 4 * 64 * 2048;
  const unsigned xoff = (unsigned)((tid >> 6) & 1) * (2 * 64 * 2048) + (unsigned)(tid >> 7) * 2048 + lane * 16;
  u32x2 bb[4];
  auto late = [&]() {
#pragma unroll
    for (int i = 0; i < 4; ++i) gload8(bb[i], (unsigned)(ft * 128 + wk * 64 + 16 * i + 4 * g) * 2, (const char*)b1);
  };
  STAMPIT(ws, 2, 0);
  gemm_main<true, G1_NS, 32, WIN1, 4>(late, (LDS_AS char*)dsm, (const char*)w1 + ft * 256, F_ * 2, Xg, xoff, xoff + 64 * 2048, acc, w,
                                      lane, rot, cnt_addr, ws);
  STAMPIT(ws, 2, 2);
  asm volatile("s_waitcnt vmcnt(0)" : "+v"(bb[0]), "+v"(bb[1]), "+v"(bb[2]), "+v"(bb[3])::"memory");
  f32x4 fin[4][4];
  kpair_reduce<8>((LDS_AS char*)dsm, acc, fin, w, lane);
#pragma unroll
  for (int i = 0; i < 2; ++i) {
    float bias[8] = {bflo(bb[2 * i][0]), bfhi(bb[2 * i][0]), bflo(bb[2 * i][1]), bfhi(bb[2 * i][1]),
                     bflo(bb[2 * i + 1][0]), bfhi(bb[2 * i + 1][0]), bflo(bb[2 * i + 1][1]), bfhi(bb[2 * i + 1][1])};
#pragma unroll
    for (int n = 0; n < 4; ++n) {
      u32x4 o;
#pragma unroll
      for (int e = 0; e < 4; ++e) {
        const f32x4 a = fin[2 * i + (e >> 1)][n];
        const f32x2 t = (f32x2){a[2 * (e & 1)], a[2 * (e & 1) + 1]} + (f32x2){bias[2 * e], bias[2 * e + 1]};
        const f32x2 z = t * ((t * t) * -0.10294324068f + -2.3022081985378543f);
        const f32x2 den = (f32x2){__builtin_amdgcn_exp2f(z[0]), __builtin_amdgcn_exp2f(z[1])} + 1.0f;
        const f32x2 r = t * (f32x2){__builtin_amdgcn_rcpf(den[0]), __builtin_amdgcn_rcpf(den[1])};
        o[e] = pack_bf16(r[0], r[1]);
      }
      const int dblk = dt * 4 + (w & 1) * 2 + (n >> 1);
      ((u32x4*)(ws + WS_UA))[((size_t)(dblk * 112 + ft * 4 + wk * 2 + i) * 2 + (n & 1)) * 64 + lane] = o;
    }
  }
  STAMPIT(ws, 2, 3);
}

__global__ __launch_bounds__(256, 2) void k_gemm2(const u16* __restrict__ w2, const u16* __restrict__ b2, const u16* __restrict__ x,
                                                  char* __restrict__ ws, u16* __restrict__ out) {
  extern __shared__ __attribute__((aligned(16))) char dsm[];
  const int b = blockIdx.x, xcd = b & 7, j = b >> 3;
  const int dt = (xcd & 1) * 4 + (j & 3), stl = (xcd >> 1) * 8 + (j >> 2);
  const int rot = ROT2M * (j & 3) + (j >> 2);
  const int tid = threadIdx.x, lane = tid & 63;
  const int w = __builtin_amdgcn_readfirstlane(tid >> 6);
  STAMPIT(ws, 3, 0);
  const unsigned cnt_addr = (unsigned)(size_t)(LDS_AS char*)dsm + G2_CNT;
  if (tid == 0) *(LDS_AS u32x2*)((LDS_AS char*)dsm + G2_CNT) = (u32x2){0u, 0u};
  __syncthreads();
  const int g = lane >> 4, n = lane & 15, q = (lane >> 2) & 3, p = lane & 3, wk = w >> 1;
  const int d0 = dt * 128 + (w & 1) * 64 + 8 * g;
  const int sbase = stl * 64 + wk * 32;
  u32x4 xv[2][2]; u32x2 stv[2]; unsigned bv[2];
  auto late = [&]() {
    {
      const char* ya = ws + WS_YA + (size_t)(dt * 128 + w * 32) * (S_ * 2) + stl * 128;
      const unsigned ldsy = (unsigned)(size_t)(LDS_AS char*)dsm + G2_YA + w * 4096;
      const int r8 = lane >> 3, cp = lane & 7;
      const unsigned off = (unsigned)r8 * (S_ * 2) + 32 * ((cp >> 1) ^ ((r8 >> 1) & 3)) + 16 * (cp & 1);
#pragma unroll
      for (int i = 0; i < 4; ++i) glds16<0, false>(off, ya + (size_t)i * 8 * (S_ * 2), ldsy + i * 1024);
    }
#pragma unroll
    for (int nt = 0; nt < 2; ++nt) {
      const int s = sbase + 16 * nt + n;
      gload16<0, true>(xv[nt][0], (unsigned)(s * D_ + d0) * 2, (const char*)x);
      gload16<64, true>(xv[nt][1], (unsigned)(s * D_ + d0) * 2, (const char*)x);
      gload8(stv[nt], (unsigned)s * 8, ws + WS_STAT);
      gload2(bv[nt], (unsigned)s * 2, (const char*)b2);
    }
  };
  f32x4 acc[4][4];
#pragma unroll
  for (int m = 0; m < 4; ++m)
#pragma unroll
    for (int nn = 0; nn < 4; ++nn) acc[m][nn] = (f32x4){0, 0, 0, 0};
  const char* Xg = ws + WS_UA + (size_t)dt * 4 * 112 * 2048;
  const unsigned xoff = (unsigned)((tid >> 6) & 1) * (2 * 112 * 2048) + (unsigned)(tid >> 7) * 2048 + lane * 16;
  gemm_main<false, G2_NS, 28, WIN2, 12>(late, (LDS_AS char*)dsm, (const char*)w2 + stl * 128, S_ * 2, Xg, xoff, xoff + 112 * 2048,
                                       acc, w, lane, rot, cnt_addr, ws);
  STAMPIT(ws, 3, 2);
  asm volatile("s_waitcnt vmcnt(0)" : "+v"(xv[0][0]), "+v"(xv[0][1]), "+v"(xv[1][0]), "+v"(xv[1][1]), "+v"(stv[0]), "+v"(stv[1]),
               "+v"(bv[0]), "+v"(bv[1])::"memory");
  f32x4 fin[2][4];
  kpair_reduce<4>((LDS_AS char*)dsm, acc, fin, w, lane);
  STAMPIT(ws, 3, 3);
#pragma unroll
  for (int nt = 0; nt < 2; ++nt) {
    const int s = sbase + 16 * nt + n;
#pragma unroll
    for (int h = 0; h < 2; ++h) {
      s16x4 yv[2];
#pragma unroll
      for (int r = 0; r < 2; ++r) {
        const int row = 64 * (w & 1) + 32 * h + 8 * g + 4 * r + q;
        yv[r] = tr_read((LDS_AS char*)dsm + G2_YA + row * 128 + 32 * ((2 * wk + nt) ^ ((row >> 1) & 3)) + 8 * p);
      }
      float r[8];
#pragma unroll
      for (int e = 0; e < 8; ++e) {
        float xe = (e & 1) ? bfhi(xv[nt][h][e >> 1]) : bflo(xv[nt][h][e >> 1]);
        float ye = bf2f((unsigned)(u16)yv[e >> 2][e & 3]);
        float xn = rbf((xe - __uint_as_float(stv[nt][0])) * __uint_as_float(stv[nt][1]));
        r[e] = xe + xn * ye + (fin[nt][2 * h + (e >> 2)][e & 3] + bf2f(bv[nt]));
      }
      u32x4 o = {pack_bf16(r[0], r[1]), pack_bf16(r[2], r[3]), pack_bf16(r[4], r[5]), pack_bf16(r[6], r[7])};
      __builtin_nontemporal_store(o, (u32x4*)(out + (size_t)s * D_ + d0 + 32 * h));
    }
  }
  STAMPIT(ws, 3, 4);
}

extern "C" void kernel_launch(void* const* d_in, const int* in_sizes, int n_in, void* d_out, int out_size, void* d_ws,
                              size_t ws_size, hipStream_t stream) {
  const u16* x = (const u16*)d_in[0];
  const u16* k = (const u16*)d_in[1];
  const u16* v = (const u16*)d_in[2];
  const u16* shk = (const u16*)d_in[3];
  const u16* ssmk = (const u16*)d_in[4];
  const u16* ssmD = (const u16*)d_in[5];
  const u16* w1 = (const u16*)d_in[7];
  const u16* b1 = (const u16*)d_in[8];
  const u16* w2 = (const u16*)d_in[9];
  const u16* b2 = (const u16*)d_in[10];
  char* ws = (char*)d_ws;
  (void)hipFuncSetAttribute((const void*)k_gemm1, hipFuncAttributeMaxDynamicSharedMemorySize, G1_LDS);
  (void)hipFuncSetAttribute((const void*)k_gemm2, hipFuncAttributeMaxDynamicSharedMemorySize, G2_LDS);
  k_prologue<<<256, 576, 0, stream>>>(x, k, v, shk, w1, ws);
  k_gemm1<<<512, 256, G1_LDS, stream>>>(w1, b1, w2, ssmk, ssmD, ws);
  k_gemm2<<<256, 256, G2_LDS, stream>>>(w2, b2, x, ws, (u16*)d_out);
}
```

```cpp
#include <hip/hip_runtime.h>
#include <hip/hip_bf16.h>
#include <utility>

typedef short    s16x8 __attribute__((ext_vector_type(8)));
typedef short    s16x4 __attribute__((ext_vector_type(4)));
typedef float    f32x4 __attribute__((ext_vector_type(4)));
typedef float    f32x2 __attribute__((ext_vector_type(2)));
typedef unsigned u32x4 __attribute__((ext_vector_type(4)));
typedef unsigned u32x2 __attribute__((ext_vector_type(2)));
typedef unsigned short u16;
#define LDS_AS __attribute__((address_space(3)))
#define DEVINL __device__ __forceinline__

constexpr int S_ = 2048, D_ = 1024, F_ = 3584;

constexpr size_t WS_HA   = 0;
constexpr size_t WS_KVT  = WS_HA  + (size_t)S_ * D_ * 2;
constexpr size_t WS_YA   = WS_KVT + (size_t)S_ * D_ * 2;
constexpr size_t WS_UA   = WS_YA  + (size_t)S_ * D_ * 2;
constexpr size_t WS_STAT = WS_UA  + (size_t)D_ * F_ * 2;

DEVINL float bf2f(unsigned v16) { return __uint_as_float(v16 << 16); }
DEVINL float bflo(unsigned v) { return __uint_as_float(v << 16); }
DEVINL float bfhi(unsigned v) { return __uint_as_float(v & 0xffff0000u); }
DEVINL unsigned pack_bf16(float lo, float hi) {
  typedef __bf16 bf2 __attribute__((ext_vector_type(2)));
  f32x2 v = {lo, hi};
  bf2 r = __builtin_convertvector(v, bf2);
  return __builtin_bit_cast(unsigned, r);
}
DEVINL float rbf(float x) { return bflo(pack_bf16(x, x)); }

template <int N> DEVINL void wait_vm() { asm volatile("s_waitcnt vmcnt(%0)" ::"n"(N) : "memory"); }
template <int I> struct IC { static constexpr int v = I; };
template <int B, int E, typename Fn> DEVINL void static_for(Fn&& f) {
  if constexpr (B < E) { f(IC<B>{}); static_for<B + 1, E>(f); }
}
DEVINL s16x4 tr_read(const LDS_AS char* p) {
  return __builtin_amdgcn_ds_read_tr16_b64_v4i16((LDS_AS s16x4*)p);
}
template <int OFF, bool NT> DEVINL void gload16(u32x4& dst, unsigned voff, const char* sbase) {
  if constexpr (NT) asm volatile("global_load_dwordx4 %0, %1, %2 offset:%3 nt" : "=v"(dst) : "v"(voff), "s"(sbase), "n"(OFF));
  else asm volatile("global_load_dwordx4 %0, %1, %2 offset:%3" : "=v"(dst) : "v"(voff), "s"(sbase), "n"(OFF));
}
template <int OFF, bool NT> DEVINL void glds16(unsigned voff, const char* sbase, unsigned ldsaddr) {
  if constexpr (NT) asm volatile("s_mov_b32 m0, %2\n\ts_nop 0\n\tglobal_load_lds_dwordx4 %0, %1 offset:%3 nt" ::"v"(voff), "s"(sbase), "s"(ldsaddr), "n"(OFF) : "memory");
  else asm volatile("s_mov_b32 m0, %2\n\ts_nop 0\n\tglobal_load_lds_dwordx4 %0, %1 offset:%3" ::"v"(voff), "s"(sbase), "s"(ldsaddr), "n"(OFF) : "memory");
}
template <int OFF, bool NT> DEVINL void glds16_more(unsigned voff, const char* sbase) {
  if constexpr (NT) asm volatile("global_load_lds_dwordx4 %0, %1 offset:%2 nt" ::"v"(voff), "s"(sbase), "n"(OFF) : "memory");
  else asm volatile("global_load_lds_dwordx4 %0, %1 offset:%2" ::"v"(voff), "s"(sbase), "n"(OFF) : "memory");
}
DEVINL void mfma_accv(f32x4& c, s16x8 a, s16x8 b) {
  asm volatile("v_mfma_f32_16x16x32_bf16 %0, %1, %2, %0" : "+v"(c) : "v"(a), "v"(b));
}
DEVINL void gload8(u32x2& dst, unsigned voff, const char* sbase) {
  asm volatile("global_load_dwordx2 %0, %1, %2" : "=v"(dst) : "v"(voff), "s"(sbase));
}
DEVINL void gload2(unsigned& dst, unsigned voff, const char* sbase) {
  asm volatile("global_load_ushort %0, %1, %2" : "=v"(dst) : "v"(voff), "s"(sbase));
}
template <typename T> DEVINL T ldnt(const void* p) { return __builtin_nontemporal_load((const T*)p); }
template <int CTRL, int RM, bool BC> DEVINL float dpp0(float v) {
  return __builtin_bit_cast(float, __builtin_amdgcn_update_dpp(0, __builtin_bit_cast(int, v), CTRL, RM, 0xf, BC));
}
DEVINL float wave_total(float v) {
  v += dpp0<0x111, 0xf, true>(v); v += dpp0<0x112, 0xf, true>(v); v += dpp0<0x114, 0xf, true>(v); v += dpp0<0x118, 0xf, true>(v);
  v += dpp0<0x142, 0xa, false>(v); v += dpp0<0x143, 0xc, false>(v);
  return __builtin_bit_cast(float, __builtin_amdgcn_readlane(__builtin_bit_cast(int, v), 63));
}
template <bool NT> DEVINL void touch1(unsigned& dummy, unsigned voff, const char* base) {
  if constexpr (NT) asm volatile("global_load_dword %0, %1, %2 nt" : "+v"(dummy) : "v"(voff), "s"(base));
  else asm volatile("global_load_dword %0, %1, %2" : "+v"(dummy) : "v"(voff), "s"(base));
}
template <bool NT> DEVINL void touch_lines(const char* base, unsigned voff, unsigned stride, int n) {
  unsigned dummy = 0u;
  for (int i = 0; i < n; ++i) { touch1<NT>(dummy, voff, base); voff += stride; }
  asm volatile("s_waitcnt vmcnt(0)" : "+v"(dummy)::"memory");
}
DEVINL void pin4(u32x4& a, u32x4& b, u32x4& c, u32x4& d) { asm volatile("" : "+v"(a), "+v"(b), "+v"(c), "+v"(d)); }
DEVINL s16x8 cat8(s16x4 a, s16x4 b) { return __builtin_shufflevector(a, b, 0, 1, 2, 3, 4, 5, 6, 7); }

__global__ __launch_bounds__(512) void k_prologue(const u16* __restrict__ x, const u16* __restrict__ kin,
                                                  const u16* __restrict__ vin, const u16* __restrict__ shk,
                                                  char* __restrict__ ws) {
  __shared__ __attribute__((aligned(16))) u16 sh[8 * 1024];
  const int b = blockIdx.x, tid = threadIdx.x, lane = tid & 63;
  const int w = __builtin_amdgcn_readfirstlane(tid >> 6);
  const int s0 = b * 8;
  const u16* xr = x + (size_t)(s0 + w) * D_;
  u32x4 xa = ldnt<u32x4>(xr + lane * 8);
  u32x4 xb = ldnt<u32x4>(xr + 512 + lane * 8);
  const int ks0 = (b >> 3) * 64 + w * 8, kd = (b & 7) * 128 + 2 * lane;
  unsigned kk[11], vv[8];
#pragma unroll
  for (int i = 0; i < 11; ++i) {
    int s = ks0 - 3 + i;
    kk[i] = ldnt<unsigned>(kin + (size_t)(s < 0 ? 0 : s) * D_ + kd);
    if (i < 3 && s < 0) kk[i] = 0u;
  }
#pragma unroll
  for (int i = 0; i < 8; ++i) vv[i] = ldnt<unsigned>(vin + (size_t)(ks0 + i) * D_ + kd);
  u32x4 sk = *(const u32x4*)(shk + (size_t)kd * 4);

  f32x2 f[8];
#pragma unroll
  for (int i = 0; i < 4; ++i) { f[i] = (f32x2){bflo(xa[i]), bfhi(xa[i])}; f[4 + i] = (f32x2){bflo(xb[i]), bfhi(xb[i])}; }
  unsigned hp[8];
#pragma unroll
  for (int pass = 0; pass < 2; ++pass) {
    f32x2 s = f[0];
#pragma unroll
    for (int i = 1; i < 8; ++i) s += f[i];
    const float mean = wave_total(s[0] + s[1]) * (1.0f / D_);
    f32x2 q = {0.f, 0.f};
#pragma unroll
    for (int i = 0; i < 8; ++i) { f32x2 dl = f[i] - mean; q += dl * dl; }
    const float var = wave_total(q[0] + q[1]) * (1.0f / D_);
    const float mb = rbf(mean);
    const float rinv = 1.0f / rbf(sqrtf(rbf(rbf(var) + 1e-5f)));
#pragma unroll
    for (int i = 0; i < 8; ++i) {
      f32x2 t = (f[i] - mb) * rinv;
      hp[i] = pack_bf16(t[0], t[1]);
      if (pass == 0) f[i] = (f32x2){bflo(hp[i]), bfhi(hp[i])};
    }
    if (pass == 0 && lane == 0) { f32x2 st = {mb, rinv}; *(f32x2*)(ws + WS_STAT + (size_t)(s0 + w) * 8) = st; }
  }
  *(u32x4*)(sh + w * 1024 + lane * 8) = (u32x4){hp[0], hp[1], hp[2], hp[3]};
  *(u32x4*)(sh + w * 1024 + 512 + lane * 8) = (u32x4){hp[4], hp[5], hp[6], hp[7]};
  {
    const f32x2 tp[4] = {{bflo(sk[0]), bflo(sk[2])}, {bfhi(sk[0]), bfhi(sk[2])}, {bflo(sk[1]), bflo(sk[3])}, {bfhi(sk[1]), bfhi(sk[3])}};
    f32x2 kf2[11], r[8];
#pragma unroll
    for (int i = 0; i < 11; ++i) kf2[i] = (f32x2){bflo(kk[i]), bfhi(kk[i])};
#pragma unroll
    for (int i = 0; i < 8; ++i) {
      f32x2 a = kf2[i] * tp[3];
      a += kf2[i + 1] * tp[2];
      a += kf2[i + 2] * tp[1];
      a += kf2[i + 3] * tp[0];
      r[i] = a * (f32x2){bflo(vv[i]), bfhi(vv[i])};
    }
    u32x4 o0, o1;
#pragma unroll
    for (int i = 0; i < 4; ++i) { o0[i] = pack_bf16(r[2 * i][0], r[2 * i + 1][0]); o1[i] = pack_bf16(r[2 * i][1], r[2 * i + 1][1]); }
    u32x4* dst = (u32x4*)(ws + WS_KVT + ((size_t)kd * S_ + ks0) * 2);
    dst[0] = o0; dst[S_ / 8] = o1;
  }
  __syncthreads();
  {
    unsigned c[8];
#pragma unroll
    for (int i = 0; i < 8; ++i) c[i] = *(const unsigned*)(sh + i * 1024 + 2 * tid);
    u32x4 o0, o1;
#pragma unroll
    for (int i = 0; i < 4; ++i) {
      o0[i] = __builtin_amdgcn_perm(c[2 * i + 1], c[2 * i], 0x05040100u);
      o1[i] = __builtin_amdgcn_perm(c[2 * i + 1], c[2 * i], 0x07060302u);
    }
    const int d = 2 * tid, dblk = d >> 5, n = (d >> 2) & 1, cc = 4 * ((d >> 3) & 3) + (d & 3);
    const int kstep = b >> 2, g = b & 3;
    u32x4* dst = (u32x4*)(ws + WS_HA) + ((size_t)(dblk * 64 + kstep) * 2 + n) * 64 + cc + 16 * g;
    dst[0] = o0; dst[1] = o1;
  }
}

constexpr int CV_XPOFF = 4160;
constexpr int CV_XP = 1152;
constexpr int CV_WAVE = CV_XPOFF + 4 * CV_XP;
constexpr int CV_LDS = 4 * CV_WAVE;

DEVINL void conv_wave(LDS_AS char* kf, int d, int lane, const u16* __restrict__ ssmk, const u16* __restrict__ ssmD,
                      char* __restrict__ ws) {
  LDS_AS char* xp = kf + CV_XPOFF;
  u32x4 kfv[4], kvv[4];
#pragma unroll
  for (int i = 0; i < 4; ++i) kfv[i] = ldnt<u32x4>(ssmk + (size_t)d * S_ + (i * 64 + lane) * 8);
#pragma unroll
  for (int i = 0; i < 4; ++i) kvv[i] = *((const u32x4*)(ws + WS_KVT + (size_t)d * (S_ * 2)) + i * 64 + lane);
  const float Dd = bf2f(ssmD[d]);
  if (lane < 8) *(LDS_AS unsigned*)(kf + lane * 4) = 0u;
  {
    u32x2 z = {0u, 0u};
    *(LDS_AS u32x2*)(xp + (lane >> 4) * CV_XP + (lane & 15) * 8) = z;
  }
#pragma unroll
  for (int i = 0; i < 4; ++i) *(LDS_AS u32x4*)(kf + 32 + (i * 64 + lane) * 16) = kfv[i];
#pragma unroll
  for (int i = 0; i < 4; ++i) {
    const int sb = i * 64 + lane;
    unsigned p0 = __builtin_amdgcn_perm(kvv[i][2], kvv[i][0], 0x05040100u);
    unsigned p1 = __builtin_amdgcn_perm(kvv[i][2], kvv[i][0], 0x07060302u);
    unsigned p2 = __builtin_amdgcn_perm(kvv[i][3], kvv[i][1], 0x05040100u);
    unsigned p3 = __builtin_amdgcn_perm(kvv[i][3], kvv[i][1], 0x07060302u);
    *(LDS_AS unsigned*)(xp + 0 * CV_XP + 128 + sb * 4) = p0;
    *(LDS_AS unsigned*)(xp + 1 * CV_XP + 128 + sb * 4) = p1;
    *(LDS_AS unsigned*)(xp + 2 * CV_XP + 128 + sb * 4) = p2;
    *(LDS_AS unsigned*)(xp + 3 * CV_XP + 128 + sb * 4) = p3;
  }
  __builtin_amdgcn_s_waitcnt(0xC07F);
  __builtin_amdgcn_wave_barrier();
  const int g = lane >> 4, q = (lane >> 2) & 3, p = lane & 3, n = lane & 15;
  s16x8 af[16];
  {
    const LDS_AS char* a0 = kf + 2 * (4 * (8 * g + 7 - q) + 4 + 4 * p);
#pragma unroll
    for (int st = 0; st < 16; ++st) {
      s16x4 lo = tr_read(a0 + st * 256);
      s16x4 hi = tr_read(a0 + st * 256 - 32);
      af[st] = cat8(lo, hi);
    }
  }
  const LDS_AS char* b0 = xp + 2 * (64 + 4 * n - 8 * g - 4);
  const int src = ((lane >= 16) ? (lane - 16) : (lane == 0 ? 63 : lane + 47)) * 4;
  float prev0 = 0.f, prev1 = 0.f, prev2 = 0.f;
  u32x2* yrow = (u32x2*)(ws + WS_YA + (size_t)d * (S_ * 2)) + 4 * n + g;
  f32x4 acc[4][8];
#pragma unroll
  for (int ph = 0; ph < 4; ++ph)
#pragma unroll
    for (int nu = 0; nu < 8; ++nu) acc[ph][nu] = (f32x4){0.f, 0.f, 0.f, 0.f};
#pragma unroll
  for (int beta = -1; beta <= 14; ++beta) {
#pragma unroll
    for (int ph = 0; ph < 4; ++ph) {
      const LDS_AS char* bp = b0 + ph * CV_XP + beta * 64;
      s16x4 lo = *(const LDS_AS s16x4*)(bp);
      s16x4 hi = *(const LDS_AS s16x4*)(bp + 8);
      s16x8 bf = cat8(lo, hi);
#pragma unroll
      for (int nu = 0; nu < 8; ++nu) {
        const int step = 2 * nu - beta;
        if (step >= 0 && step <= 15)
          acc[ph][nu] = __builtin_amdgcn_mfma_f32_16x16x32_bf16(af[step], bf, acc[ph][nu], 0, 0, 0);
      }
    }
  }
#pragma unroll
  for (int nu = 0; nu < 8; ++nu) {
    float c0 = acc[1][nu][3] + acc[2][nu][2] + acc[3][nu][1];
    float c1 = acc[2][nu][3] + acc[3][nu][2];
    float c2 = acc[3][nu][3];
    float p0 = __int_as_float(__builtin_amdgcn_ds_bpermute(src, __float_as_int(c0)));
    float p1 = __int_as_float(__builtin_amdgcn_ds_bpermute(src, __float_as_int(c1)));
    float p2 = __int_as_float(__builtin_amdgcn_ds_bpermute(src, __float_as_int(c2)));
    float a0 = (lane == 0) ? prev0 : p0, a1 = (lane == 0) ? prev1 : p1, a2 = (lane == 0) ? prev2 : p2;
    prev0 = p0; prev1 = p1; prev2 = p2;
    float y[4];
    y[0] = acc[0][nu][0] + a0;
    y[1] = acc[0][nu][1] + acc[1][nu][0] + a1;
    y[2] = acc[0][nu][2] + acc[1][nu][1] + acc[2][nu][0] + a2;
    y[3] = acc[0][nu][3] + acc[1][nu][2] + acc[2][nu][1] + acc[3][nu][0];
#pragma unroll
    for (int r = 0; r < 4; ++r) {
      float kvv_ = bf2f(*(const LDS_AS u16*)(xp + r * CV_XP + 128 + 2 * (64 * nu + 4 * n + g)));
      y[r] += kvv_ * Dd;
    }
    u32x2 o = {pack_bf16(y[0], y[1]), pack_bf16(y[2], y[3])};
    yrow[64 * nu] = o;
  }
}

#define SB() __builtin_amdgcn_sched_barrier(0)
template <bool G1, int NS, int NST, int NLATE, typename Hook>
DEVINL void gemm_main(Hook&& late_loads  ,
                      LDS_AS char* smem, const char* __restrict__ Wg  , const unsigned wrow  ,
                      const char* __restrict__ Xg  , unsigned xoff0, unsigned xoff1  ,
                      f32x4 (&acc)[G1 ? 8 : 4][4], int w, int lane, int rot  ) {
  constexpr int NWT = G1 ? 8 : 4;
  constexpr int ROWB = NWT * 32;
  constexpr int STB = 16384;
  constexpr int KROWS = STB / ROWB;
  constexpr int KW = KROWS / 64;
  constexpr int NX = 4 * KW;
  constexpr int OPS = 4 + NX;
  constexpr int NF = 8;
  constexpr int LA = 3;
  static_assert(NS == 4 && NST % NS == 0 && NS * NX * 4 + NWT * 16 <= 192, "register budget: in-flight X fragments must never be spilled");
  const int g = lane >> 4, q = (lane >> 2) & 3, p = lane & 3, wk = w >> 1;
  unsigned woff[4];
#pragma unroll
  for (int i = 0; i < 4; ++i) {
    int row, cp, key;
    if (G1) { row = 16 * w + 4 * i + (lane >> 4); cp = lane & 15; key = (row & 3) | (((row >> 3) & 1) << 2); }
    else    { row = 32 * w + 8 * i + (lane >> 3); cp = lane & 7;  key = (row >> 1) & 3; }
    woff[i] = (unsigned)row * wrow + 32 * ((cp >> 1) ^ key) + 16 * (cp & 1) - i * 1024;
  }
  const unsigned ldsw = (unsigned)(size_t)smem + w * 4096;
  unsigned roff[NWT];
#pragma unroll
  for (int m = 0; m < NWT; ++m) {
    const int row = G1 ? (8 * g + q) : (4 * g + q);
    const int key = G1 ? (q | ((g & 1) << 2)) : ((row >> 1) & 3);
    roff[m] = (row + 32 * wk) * ROWB + 32 * ((m ^ (wk * (NWT / 2))) ^ key) + 8 * p;
  }
  constexpr int RSTEP = G1 ? 4 * ROWB : 16 * ROWB;
  u32x4 xf[NS][NX];
  s16x4 wlo[4], whi[4];

  const char* wp[NS];
  const char* xp[NS];
#pragma unroll
  for (int i = 0; i < NS; ++i) {
    const int st = (i + rot) & (NS - 1);
    wp[i] = Wg + (size_t)st * KROWS * wrow;
    xp[i] = Xg + (size_t)st * (KW * 4096) + 4096;
  }
  auto issue1 = [&](auto SL, auto OP) {
    constexpr int sl = decltype(SL)::v, op = decltype(OP)::v;
    if constexpr (op == 0) glds16<0, false>(woff[0], wp[sl], ldsw + sl * STB);
    else if constexpr (op < 4) glds16_more<op * 1024, false>(woff[op], wp[sl]);
    else {
      constexpr int xi = op - 4, ki = xi >> 2, h = (xi >> 1) & 1, n = xi & 1;
      gload16<ki * 4096 + n * 1024 - 4096, false>(xf[sl][xi], h ? xoff1 : xoff0, xp[sl]);
    }
    if constexpr (op == 3) wp[sl] += (size_t)NS * KROWS * wrow;
    if constexpr (op == 4 + NX - 1) xp[sl] += (size_t)NS * (KW * 4096);
  };
  auto readh = [&](auto SL, auto J, auto HF) {
    constexpr int sl = decltype(SL)::v, j = decltype(J)::v, half = decltype(HF)::v;
    constexpr int ki = j / NWT, m = j % NWT, r = j & 3;
    const LDS_AS char* a = smem + sl * STB + ki * 64 * ROWB + roff[m] + half * RSTEP;
    if constexpr (half == 0) wlo[r] = tr_read(a); else whi[r] = tr_read(a);
  };
  auto mma1 = [&](auto SL, auto J, auto N) {
    constexpr int sl = decltype(SL)::v, j = decltype(J)::v, n = decltype(N)::v;
    constexpr int ki = j / NWT, m = j % NWT, r = j & 3;
    s16x8 xv = __builtin_bit_cast(s16x8, xf[sl][ki * 4 + n]);
    s16x8 wv = cat8(wlo[r], whi[r]);
    if (G1) mfma_accv(acc[m][n], wv, xv);
    else    mfma_accv(acc[m][n], xv, wv);
  };
  auto pin = [&](auto SL) {
    constexpr int sl = decltype(SL)::v;
    pin4(xf[sl][0], xf[sl][1], xf[sl][2], xf[sl][3]);
    if constexpr (NX == 8) pin4(xf[sl][4], xf[sl][5], xf[sl][6], xf[sl][7]);
  };
  auto stage = [&](auto I, auto ISSUE, auto WAITN, auto EXTRA) {
    constexpr int i = decltype(I)::v, nx = (i + 1) % NS, pv = (i + NS - 1) % NS;
    constexpr int waitn = decltype(WAITN)::v;
    constexpr bool do_issue = decltype(ISSUE)::v;
    static_for<0, NF>([&](auto J) {
      constexpr int j = decltype(J)::v, ja = j + LA;
      if constexpr (waitn >= 0 && j == NF - LA) {
        wait_vm<waitn * OPS + decltype(EXTRA)::v + (do_issue ? NX : 0)>();
        pin(IC<nx>{});
        __builtin_amdgcn_s_barrier();
        SB();
      }
      mma1(IC<i>{}, J, IC<0>{}); SB();
      if constexpr (ja < NF) readh(IC<i>{}, IC<ja>{}, IC<0>{});
      else if constexpr (waitn >= 0) readh(IC<nx>{}, IC<ja - NF>{}, IC<0>{});
      SB();
      mma1(IC<i>{}, J, IC<1>{}); SB();
      if constexpr (ja < NF) readh(IC<i>{}, IC<ja>{}, IC<1>{});
      else if constexpr (waitn >= 0) readh(IC<nx>{}, IC<ja - NF>{}, IC<1>{});
      SB();
      mma1(IC<i>{}, J, IC<2>{}); SB();
      if constexpr (do_issue) {
        if constexpr (j >= 1 && j <= 4) issue1(IC<pv>{}, IC<4 + (j - 1) * KW>{});
        if constexpr (j >= NF - LA) issue1(IC<pv>{}, IC<(j == 5) ? 0 : j - 4>{});
      }
      SB();
      mma1(IC<i>{}, J, IC<3>{}); SB();
      if constexpr (do_issue) {
        if constexpr (KW == 2 && j >= 1 && j <= 4) issue1(IC<pv>{}, IC<4 + (j - 1) * KW + 1>{});
        if constexpr (j == NF - LA) issue1(IC<pv>{}, IC<1>{});
      }
      SB();
    });
  };

  static_for<0, NS - 1>([&](auto I) {
    static_for<0, NX>([&](auto OP) { issue1(I, IC<4 + decltype(OP)::v>{}); });
    static_for<0, 4>([&](auto OP) { issue1(I, OP); });
  });
  wait_vm<(NS - 2) * OPS>();
  pin(IC<0>{});
  __builtin_amdgcn_s_barrier();
  SB();
  static_for<0, LA>([&](auto J) { readh(IC<0>{}, J, IC<0>{}); readh(IC<0>{}, J, IC<1>{}); });
  SB();

  for (int tb = 0; tb < NST - NS; tb += NS) {
    static_for<0, NS>([&](auto I) { stage(I, IC<1>{}, IC<NS - 3>{}, IC<0>{}); });
  }
  static_for<0, NS>([&](auto I) {
    constexpr int i = decltype(I)::v;
    constexpr int outst = (i == NS - 1) ? -1 : ((i == 0) ? (NS - 3) : (NS - 2 - i));
    stage(I, IC<(i == 0)>{}, IC<outst>{}, IC<(i == 0) ? 0 : NLATE>{});
    if constexpr (i == 0) { late_loads(); SB(); }
  });
  asm volatile("s_nop 15" ::: "memory");
  SB();
}

template <int NWT>
DEVINL void kpair_reduce(LDS_AS char* smem, f32x4 (&acc)[NWT][4], f32x4 (&fin)[NWT / 2][4], int w, int lane) {
  constexpr int H = NWT / 2;
  __syncthreads();
  LDS_AS char* mine = smem + (w * H * 4 * 64 + lane) * 16;
  LDS_AS char* other = smem + ((w ^ 2) * H * 4 * 64 + lane) * 16;
#pragma unroll
  for (int m = 0; m < H; ++m)
#pragma unroll
    for (int n = 0; n < 4; ++n) *(LDS_AS f32x4*)(mine + (m * 4 + n) * 1024) = acc[H + m][n];
  __syncthreads();
#pragma unroll
  for (int m = 0; m < H; ++m)
#pragma unroll
    for (int n = 0; n < 4; ++n) fin[m][n] = acc[m][n] + *(const LDS_AS f32x4*)(other + (m * 4 + n) * 1024);
}

constexpr int G1_NS = 4, G2_NS = 4;
constexpr int G2_YA = G2_NS * 16384;
constexpr int G1_LDS = G1_NS * 16384;
constexpr int G2_LDS = 98304;
static_assert(CV_LDS <= G1_LDS && G2_YA + 16384 <= G2_LDS, "");

__global__ __launch_bounds__(256, 2) void k_gemm1(const u16* __restrict__ w1, const u16* __restrict__ b1,
                                                  const u16* __restrict__ w2, const u16* __restrict__ ssmk,
                                                  const u16* __restrict__ ssmD, char* __restrict__ ws) {
  extern __shared__ __attribute__((aligned(16))) char dsm[];
  const int b = blockIdx.x & 255, xcd = b & 7, j = b >> 3;
  const int tid = threadIdx.x, lane = tid & 63;
  const int w = __builtin_amdgcn_readfirstlane(tid >> 6);
  if (blockIdx.x >= 256) {
    conv_wave((LDS_AS char*)dsm + w * CV_WAVE, (xcd * 32 + j) * 4 + w, lane, ssmk, ssmD, ws);
    return;
  }
  if (j >= 28) {
    const int wv = (b - 224) * 4 + w;
    touch_lines<false>((const char*)w1 + (size_t)10 * 128 * 8192 + (size_t)wv * 8192, lane * 128, 128 * 8192, 4);
    touch_lines<false>((const char*)w2 + (size_t)wv * 8192, lane * 128, 128 * 8192, 14);
    return;
  }
  const int dt = (xcd & 1) * 4 + (j & 3), ft = (xcd >> 1) * 7 + (j >> 2);
  const int rot = (j & 3) + (j >> 2);
  const int g = lane >> 4, wk = w >> 1;
  f32x4 acc[8][4];
#pragma unroll
  for (int m = 0; m < 8; ++m)
#pragma unroll
    for (int n = 0; n < 4; ++n) acc[m][n] = (f32x4){0, 0, 0, 0};
  const char* Xg = ws + WS_HA + (size_t)dt * 4 * 64 * 2048;
  const unsigned xoff = (unsigned)((tid >> 6) & 1) * (2 * 64 * 2048) + (unsigned)(tid >> 7) * 2048 + lane * 16;
  u32x2 bb[4];
  auto late = [&]() {
#pragma unroll
    for (int i = 0; i < 4; ++i) gload8(bb[i], (unsigned)(ft * 128 + wk * 64 + 16 * i + 4 * g) * 2, (const char*)b1);
  };
  gemm_main<true, G1_NS, 32, 4>(late, (LDS_AS char*)dsm, (const char*)w1 + ft * 256, F_ * 2, Xg, xoff, xoff + 64 * 2048, acc, w, lane, rot);
  asm volatile("s_waitcnt vmcnt(0)" : "+v"(bb[0]), "+v"(bb[1]), "+v"(bb[2]), "+v"(bb[3])::"memory");
  f32x4 fin[4][4];
  kpair_reduce<8>((LDS_AS char*)dsm, acc, fin, w, lane);
#pragma unroll
  for (int i = 0; i < 2; ++i) {
    float bias[8] = {bflo(bb[2 * i][0]), bfhi(bb[2 * i][0]), bflo(bb[2 * i][1]), bfhi(bb[2 * i][1]),
                     bflo(bb[2 * i + 1][0]), bfhi(bb[2 * i + 1][0]), bflo(bb[2 * i + 1][1]), bfhi(bb[2 * i + 1][1])};
#pragma unroll
    for (int n = 0; n < 4; ++n) {
      u32x4 o;
#pragma unroll
      for (int e = 0; e < 4; ++e) {
        const f32x4 a = fin[2 * i + (e >> 1)][n];
        const f32x2 t = (f32x2){a[2 * (e & 1)], a[2 * (e & 1) + 1]} + (f32x2){bias[2 * e], bias[2 * e + 1]};
        const f32x2 z = t * ((t * t) * -0.10294324068f + -2.3022081985378543f);
        const f32x2 den = (f32x2){__builtin_amdgcn_exp2f(z[0]), __builtin_amdgcn_exp2f(z[1])} + 1.0f;
        const f32x2 r = t * (f32x2){__builtin_amdgcn_rcpf(den[0]), __builtin_amdgcn_rcpf(den[1])};
        o[e] = pack_bf16(r[0], r[1]);
      }
      const int dblk = dt * 4 + (w & 1) * 2 + (n >> 1);
      ((u32x4*)(ws + WS_UA))[((size_t)(dblk * 112 + ft * 4 + wk * 2 + i) * 2 + (n & 1)) * 64 + lane] = o;
    }
  }
}

__global__ __launch_bounds__(256, 2) void k_gemm2(const u16* __restrict__ w2, const u16* __restrict__ b2, const u16* __restrict__ x,
                                                  char* __restrict__ ws, u16* __restrict__ out) {
  extern __shared__ __attribute__((aligned(16))) char dsm[];
  const int b = blockIdx.x, xcd = b & 7, j = b >> 3;
  const int dt = (xcd & 1) * 4 + (j & 3), stl = (xcd >> 1) * 8 + (j >> 2);
  const int rot = (j & 3) + (j >> 2);
  const int tid = threadIdx.x, lane = tid & 63;
  const int w = __builtin_amdgcn_readfirstlane(tid >> 6);
  const int g = lane >> 4, n = lane & 15, q = (lane >> 2) & 3, p = lane & 3, wk = w >> 1;
  const int d0 = dt * 128 + (w & 1) * 64 + 8 * g;
  const int sbase = stl * 64 + wk * 32;
  u32x4 xv[2][2]; u32x2 stv[2]; unsigned bv[2];
  auto late = [&]() {
    {
      const char* ya = ws + WS_YA + (size_t)(dt * 128 + w * 32) * (S_ * 2) + stl * 128;
      const unsigned ldsy = (unsigned)(size_t)(LDS_AS char*)dsm + G2_YA + w * 4096;
      const int r8 = lane >> 3, cp = lane & 7;
      const unsigned off = (unsigned)r8 * (S_ * 2) + 32 * ((cp >> 1) ^ ((r8 >> 1) & 3)) + 16 * (cp & 1);
#pragma unroll
      for (int i = 0; i < 4; ++i) glds16<0, false>(off, ya + (size_t)i * 8 * (S_ * 2), ldsy + i * 1024);
    }
#pragma unroll
    for (int nt = 0; nt < 2; ++nt) {
      const int s = sbase + 16 * nt + n;
      gload16<0, true>(xv[nt][0], (unsigned)(s * D_ + d0) * 2, (const char*)x);
      gload16<64, true>(xv[nt][1], (unsigned)(s * D_ + d0) * 2, (const char*)x);
      gload8(stv[nt], (unsigned)s * 8, ws + WS_STAT);
      gload2(bv[nt], (unsigned)s * 2, (const char*)b2);
    }
  };
  f32x4 acc[4][4];
#pragma unroll
  for (int m = 0; m < 4; ++m)
#pragma unroll
    for (int nn = 0; nn < 4; ++nn) acc[m][nn] = (f32x4){0, 0, 0, 0};
  const char* Xg = ws + WS_UA + (size_t)dt * 4 * 112 * 2048;
  const unsigned xoff = (unsigned)((tid >> 6) & 1) * (2 * 112 * 2048) + (unsigned)(tid >> 7) * 2048 + lane * 16;
  gemm_main<false, G2_NS, 28, 12>(late, (LDS_AS char*)dsm, (const char*)w2 + stl * 128, S_ * 2, Xg, xoff, xoff + 112 * 2048, acc, w, lane, rot);
  asm volatile("s_waitcnt vmcnt(0)" : "+v"(xv[0][0]), "+v"(xv[0][1]), "+v"(xv[1][0]), "+v"(xv[1][1]), "+v"(stv[0]), "+v"(stv[1]),
               "+v"(bv[0]), "+v"(bv[1])::"memory");
  f32x4 fin[2][4];
  kpair_reduce<4>((LDS_AS char*)dsm, acc, fin, w, lane);
#pragma unroll
  for (int nt = 0; nt < 2; ++nt) {
    const int s = sbase + 16 * nt + n;
#pragma unroll
    for (int h = 0; h < 2; ++h) {
      s16x4 yv[2];
#pragma unroll
      for (int r = 0; r < 2; ++r) {
        const int row = 64 * (w & 1) + 32 * h + 8 * g + 4 * r + q;
        yv[r] = tr_read((LDS_AS char*)dsm + G2_YA + row * 128 + 32 * ((2 * wk + nt) ^ ((row >> 1) & 3)) + 8 * p);
      }
      float r[8];
#pragma unroll
      for (int e = 0; e < 8; ++e) {
        float xe = (e & 1) ? bfhi(xv[nt][h][e >> 1]) : bflo(xv[nt][h][e >> 1]);
        float ye = bf2f((unsigned)(u16)yv[e >> 2][e & 3]);
        float xn = rbf((xe - __uint_as_float(stv[nt][0])) * __uint_as_float(stv[nt][1]));
        r[e] = xe + xn * ye + (fin[nt][2 * h + (e >> 2)][e & 3] + bf2f(bv[nt]));
      }
      u32x4 o = {pack_bf16(r[0], r[1]), pack_bf16(r[2], r[3]), pack_bf16(r[4], r[5]), pack_bf16(r[6], r[7])};
      __builtin_nontemporal_store(o, (u32x4*)(out + (size_t)s * D_ + d0 + 32 * h));
    }
  }
}

extern "C" void kernel_launch(void* const* d_in, const int* in_sizes, int n_in, void* d_out, int out_size, void* d_ws,
                              size_t ws_size, hipStream_t stream) {
  const u16* x = (const u16*)d_in[0];
  const u16* k = (const u16*)d_in[1];
  const u16* v = (const u16*)d_in[2];
  const u16* shk = (const u16*)d_in[3];
  const u16* ssmk = (const u16*)d_in[4];
  const u16* ssmD = (const u16*)d_in[5];
  const u16* w1 = (const u16*)d_in[7];
  const u16* b1 = (const u16*)d_in[8];
  const u16* w2 = (const u16*)d_in[9];
  const u16* b2 = (const u16*)d_in[10];
  char* ws = (char*)d_ws;
  (void)hipFuncSetAttribute((const void*)k_gemm1, hipFuncAttributeMaxDynamicSharedMemorySize, G1_LDS);
  (void)hipFuncSetAttribute((const void*)k_gemm2, hipFuncAttributeMaxDynamicSharedMemorySize, G2_LDS);
  k_prologue<<<256, 512, 0, stream>>>(x, k, v, shk, ws);
  k_gemm1<<<512, 256, G1_LDS, stream>>>(w1, b1, w2, ssmk, ssmD, ws);
  k_gemm2<<<256, 256, G2_LDS, stream>>>(w2, b2, x, ws, (u16*)d_out);
}
```
